```python
import torch

def _softmax(x, axis=-1):
    return torch.softmax(x, dim=axis)
def _gelu(x):
    return torch.nn.functional.gelu(x, approximate="tanh")
def _silu(x):
    return torch.nn.functional.silu(x)
def _sigmoid(x):
    return torch.sigmoid(x)
def _layernorm(x, axis=-1, eps=1e-5):
    m = x.mean(dim=axis, keepdim=True)
    v = x.var(dim=axis, keepdim=True, unbiased=False)
    return (x - m) / torch.sqrt(v + eps)
def _scatter_add(tgt, idx, src, axis=0):
    return tgt.clone().index_add_(axis, idx, src)
def _segment_sum(src, seg, n):
    out = torch.zeros((n,) + src.shape[1:], dtype=src.dtype)
    return out.index_add_(0, seg, src)
def _pad(x, pw):
    flat = tuple(v for pair in reversed(pw) for v in pair)
    return torch.nn.functional.pad(x, flat)

kernel_name = 'parallel_sum__mha_block__glu_gated_swiglu__prompt'

def setup_inputs():

    _w = (lambda a: (lambda t: t.to(torch.bfloat16) if t.is_floating_point() else t)(torch.as_tensor(a)))
    def _A_setup():
        B, S, D, H = 1, 2048, 576, 32
        return {
            "x": _w((lambda *s: torch.randn(*s, dtype=torch.bfloat16) * 0.05)(B, S, D)),
            "wqkv": _w((lambda *s: torch.randn(*s, dtype=torch.bfloat16) * 0.05)(D, 3 * D)),
            "wo": _w((lambda *s: torch.randn(*s, dtype=torch.bfloat16) * 0.05)(D, D)),
        }
    _A_in = _A_setup()

    def _B_setup():
        B, S, D, F = 1, 2048, 576, 4608
        return {
            "x": _w((lambda *s: torch.randn(*s, dtype=torch.bfloat16) * 0.05)(B, S, D)),
            "w1": _w((lambda *s: torch.randn(*s, dtype=torch.bfloat16) * 0.4)(D, F) * (2.0 / D) ** 0.5),
            "w3": _w((lambda *s: torch.randn(*s, dtype=torch.bfloat16) * 0.4)(D, F) * (2.0 / D) ** 0.5),
            "w2": _w((lambda *s: torch.randn(*s, dtype=torch.bfloat16) * 0.4)(F, D) * (2.0 / F) ** 0.5),
        }
    _B_in = _B_setup()

    return {
        "A_x": _A_in["x"],
        "A_wqkv": _A_in["wqkv"],
        "A_wo": _A_in["wo"],
        "B_x": _B_in["x"],
        "B_w1": _B_in["w1"],
        "B_w3": _B_in["w3"],
        "B_w2": _B_in["w2"],
    }

def reference(A_x, A_wqkv, A_wo, B_x, B_w1, B_w3, B_w2):
    def _A_ref(x, wqkv, wo):
        B, S, D = x.shape
        H = 32
        Dh = D // H
        qkv = (lambda x, *d: x.permute(*d))((x @ wqkv).reshape(B, S, 3, H, Dh), 2, 0, 3, 1, 4)
        q, k, v = qkv[0], qkv[1], qkv[2]
        scores = torch.einsum("bhqd,bhkd->bhqk", q, k) * (Dh ** -0.5)
        p = _softmax(scores, axis=-1)
        o = torch.einsum("bhqk,bhkd->bhqd", p, v)
        return (lambda x, *d: x.permute(*d))(o, 0, 2, 1, 3).reshape(B, S, D) @ wo

    def _B_ref(x, w1, w3, w2):
        gate = torch.einsum("bsd,df->bsf", x, w1)
        up = torch.einsum("bsd,df->bsf", x, w3)
        h = _silu(gate) * up
        out = torch.einsum("bsf,fd->bsd", h, w2)
        return out

    x = A_x
    xn = _layernorm(x, axis=-1)
    return x + _A_ref(xn, A_wqkv, A_wo) + _B_ref(xn, B_w1, B_w3, B_w2)
```

```cpp
#include <hip/hip_runtime.h>
#include <hip/hip_bf16.h>
#include <stdint.h>

typedef __attribute__((ext_vector_type(4))) short s16x4;
typedef __attribute__((ext_vector_type(8))) short s16x8;
typedef __attribute__((ext_vector_type(4))) float f32x4;
typedef __attribute__((ext_vector_type(16))) float f32x16;
typedef __attribute__((ext_vector_type(2))) unsigned u32x2;
typedef __attribute__((ext_vector_type(4))) unsigned u32x4;
typedef __attribute__((ext_vector_type(2))) __bf16 bf16x2;
typedef unsigned short u16;
typedef __attribute__((address_space(3))) s16x4* lds_s16x4_ptr;
typedef __attribute__((address_space(3))) char* lds_char_ptr;

constexpr int S_ = 2048, D_ = 576, H_ = 32, DH_ = 18, F_ = 4608, QKV_ = 1728;

#define DI __device__ __forceinline__

DI float bf2f(u16 v) { return __uint_as_float(((unsigned)v) << 16); }
DI u16 f2bf(float f) { return __builtin_bit_cast(u16, (__bf16)f); }
DI float bfr(float f) { return bf2f(f2bf(f)); }
DI unsigned pk2(float a, float b) { bf16x2 v = {(__bf16)a, (__bf16)b}; return __builtin_bit_cast(unsigned, v); }

DI void glds16(const void* gsrc, unsigned lds_dst) {
  unsigned keep;
  asm volatile("s_mov_b32 %0, m0\n\ts_mov_b32 m0, %2\n\ts_nop 0\n\tglobal_load_lds_dwordx4 %1, off\n\ts_mov_b32 m0, %0"
               : "=&s"(keep) : "v"(gsrc), "s"(lds_dst) : "memory");
}
template <int N> DI void wait_vm_bar() { asm volatile("s_waitcnt vmcnt(%0)\n\ts_barrier" ::"i"(N) : "memory"); }

DI unsigned lds_base(char* smem) { return (unsigned)(uintptr_t)(lds_char_ptr)smem; }

__global__ __launch_bounds__(256) void k_ln(const u16* __restrict__ x, u16* __restrict__ xn) {
  const int row = blockIdx.x * 4 + (threadIdx.x >> 6), lane = threadIdx.x & 63;
  const u16* xr = x + (size_t)row * D_;
  u32x4 c0 = *(const u32x4*)(xr + lane * 8);
  u32x4 c1 = {0u, 0u, 0u, 0u};
  const bool two = lane < 8;
  if (two) c1 = *(const u32x4*)(xr + 512 + lane * 8);
  float v[16];
#pragma unroll
  for (int i = 0; i < 4; ++i) {
    v[2 * i] = __uint_as_float(c0[i] << 16); v[2 * i + 1] = __uint_as_float(c0[i] & 0xffff0000u);
    v[8 + 2 * i] = __uint_as_float(c1[i] << 16); v[8 + 2 * i + 1] = __uint_as_float(c1[i] & 0xffff0000u);
  }
  float s = 0.f;
#pragma unroll
  for (int i = 0; i < 16; ++i) s += v[i];
#pragma unroll
  for (int o = 32; o > 0; o >>= 1) s += __shfl_xor(s, o);
  const float mean = s * (1.0f / D_);
  float q = 0.f;
#pragma unroll
  for (int i = 0; i < 8; ++i) { float d = v[i] - mean; q += d * d; }
  if (two) {
#pragma unroll
    for (int i = 8; i < 16; ++i) { float d = v[i] - mean; q += d * d; }
  }
#pragma unroll
  for (int o = 32; o > 0; o >>= 1) q += __shfl_xor(q, o);
  const float var = bfr(q * (1.0f / D_));
  const float m = bfr(mean);
  const float sd = bfr(sqrtf(bfr(var + 1e-5f)));
  u32x4 o0, o1;
#pragma unroll
  for (int i = 0; i < 4; ++i) {
    o0[i] = pk2(bfr(v[2 * i] - m) / sd, bfr(v[2 * i + 1] - m) / sd);
    o1[i] = pk2(bfr(v[8 + 2 * i] - m) / sd, bfr(v[8 + 2 * i + 1] - m) / sd);
  }
  u16* orow = xn + (size_t)row * D_;
  *(u32x4*)(orow + lane * 8) = o0;
  if (two) *(u32x4*)(orow + 512 + lane * 8) = o1;
}

template <int NW, int NT, int KS, int NSTAGE>
struct Gemm {
  static constexpr int NG = (NT + 3) / 4;
  static constexpr int BSTEP = 4 * NG * 1024;
  static constexpr int ASTEP = NW * 2048;
  static constexpr int STAGE = KS * (BSTEP + ASTEP);
  static constexpr int NBI = KS * 4 * NG / NW;
  static constexpr int NAI = KS * 2;
  static constexpr int NGL = NBI + NAI;
  static_assert((KS * 4 * NG) % NW == 0, "B staging must divide over waves");

  template <typename ColFn>
  static DI void run(char* smem, const u16* __restrict__ A, int lda, int nstages, ColFn colptr, f32x4 (&acc)[2][NT]) {
    const int tid = threadIdx.x, lane = tid & 63;
    const int w = __builtin_amdgcn_readfirstlane(tid >> 6);
    const unsigned lds0 = lds_base(smem);
    const u16* bsrc[NBI]; int bstep[NBI];
#pragma unroll
    for (int j = 0; j < NBI; ++j) {
      const int id = w * NBI + j;
      const int ks = id / (4 * NG), r1 = id % (4 * NG), kb = r1 / NG, grp = r1 % NG;
      const int row = lane >> 3, ip = (lane & 7) >> 1, half = lane & 1;
      const int swz = ((row >> 1) & 1) | ((kb & 1) << 1);
      int c = grp * 4 + (ip ^ swz);
      c = c < NT ? c : NT - 1;
      int ld;
      const u16* p = colptr(c, ld);
      bsrc[j] = p + (size_t)(ks * 32 + kb * 8 + row) * ld + 8 * half;
      bstep[j] = KS * 32 * ld;
    }
    const u16* asrc[NAI];
#pragma unroll
    for (int j = 0; j < NAI; ++j) {
      const int ks = j >> 1, rb = j & 1;
      const int r = 16 * rb + (lane >> 2), cp = lane & 3;
      const int ch = cp ^ ((-(r >> 2)) & 3);
      asrc[j] = A + (size_t)(32 * w + r) * lda + ks * 32 + ch * 8;
    }
    unsigned ibuf = 0;
    auto issue = [&]() {
      const unsigned base = lds0 + ibuf;
#pragma unroll
      for (int j = 0; j < NBI; ++j) {
        const int id = w * NBI + j;
        glds16(bsrc[j], base + (unsigned)id * 1024u);
        bsrc[j] += bstep[j];
      }
#pragma unroll
      for (int j = 0; j < NAI; ++j) {
        const int ks = j >> 1, rb = j & 1;
        glds16(asrc[j], base + (unsigned)(KS * BSTEP + ks * ASTEP + w * 2048 + rb * 1024));
        asrc[j] += KS * 32;
      }
      ibuf = ibuf + STAGE == NSTAGE * STAGE ? 0u : ibuf + STAGE;
    };
    const int g = lane >> 4, q = (lane & 15) >> 2, p4 = lane & 3;
    const int bswz = ((q >> 1) & 1) | ((g & 1) << 1);
    int boff[4];
#pragma unroll
    for (int i = 0; i < 4; ++i) boff[i] = g * NG * 1024 + q * 128 + ((i ^ bswz) * 32) + p4 * 8;
    const int ar = lane & 15;
    const int aoff = KS * BSTEP + w * 2048 + ar * 64 + ((g ^ ((-(ar >> 2)) & 3)) * 16);

#pragma unroll
    for (int rb = 0; rb < 2; ++rb)
#pragma unroll
      for (int c = 0; c < NT; ++c) acc[rb][c] = f32x4{0.f, 0.f, 0.f, 0.f};

#pragma unroll
    for (int s = 0; s < NSTAGE - 1; ++s) if (s < nstages) issue();

    unsigned cbuf = 0;
#pragma unroll 1
    for (int s = 0; s < nstages; ++s) {
      if (s + NSTAGE - 2 < nstages) wait_vm_bar<(NSTAGE - 2) * NGL>(); else wait_vm_bar<0>();
      if (s + NSTAGE - 1 < nstages) issue();
      const char* sb = smem + cbuf;
      cbuf = cbuf + STAGE == NSTAGE * STAGE ? 0u : cbuf + STAGE;
#pragma unroll
      for (int ks = 0; ks < KS; ++ks) {
        const s16x8 a0 = *(const s16x8*)(sb + aoff + ks * ASTEP);
        const s16x8 a1 = *(const s16x8*)(sb + aoff + ks * ASTEP + 1024);
#pragma unroll
        for (int c = 0; c < NT; ++c) {
          const char* bp = sb + ks * BSTEP + boff[c & 3] + (c >> 2) * 1024;
          const s16x4 b0 = __builtin_amdgcn_ds_read_tr16_b64_v4i16((lds_s16x4_ptr)(bp));
          const s16x4 b1 = __builtin_amdgcn_ds_read_tr16_b64_v4i16((lds_s16x4_ptr)(bp + 512));
          const s16x8 b = __builtin_shufflevector(b0, b1, 0, 1, 2, 3, 4, 5, 6, 7);
          acc[0][c] = __builtin_amdgcn_mfma_f32_16x16x32_bf16(b, a0, acc[0][c], 0, 0, 0);
          acc[1][c] = __builtin_amdgcn_mfma_f32_16x16x32_bf16(b, a1, acc[1][c], 0, 0, 0);
        }
      }
    }
  }
};

typedef Gemm<8, 22, 1, 3> G1;
constexpr float QSCALE = 0.23570226039551584f * 1.4426950408889634f;

__global__ __launch_bounds__(512, 2) void k_gemm1(const u16* __restrict__ xn, const u16* __restrict__ wqkv,
                                                  const u16* __restrict__ w1, const u16* __restrict__ w3,
                                                  u16* __restrict__ hbuf, u16* __restrict__ qbuf,
                                                  u16* __restrict__ kmain, u16* __restrict__ ktail,
                                                  u16* __restrict__ vbuf) {
  extern __shared__ __attribute__((aligned(16))) char smem[];
  const int b = blockIdx.x;
  const int cgp = (b & 7) * 4 + ((b >> 3) & 3);
  const int panel = b >> 5;
  const int m0 = panel * 256, n0 = cgp * 144;
  const int qt0 = (108 * cgp) >> 5, qt1 = (108 * (cgp + 1)) >> 5;
  auto colptr = [&](int c, int& ld) -> const u16* {
    if (c < 9) { ld = F_; return w1 + n0 + 16 * c; }
    if (c < 18) { ld = F_; return w3 + n0 + 16 * (c - 9); }
    int t = qt0 + (c - 18); t = t < qt1 ? t : qt1 - 1;
    ld = QKV_; return wqkv + 16 * t;
  };
  f32x4 acc[2][22];
  G1::run(smem, xn + (size_t)m0 * D_, D_, D_ / 32, colptr, acc);

  const int lane = threadIdx.x & 63, w = threadIdx.x >> 6, g = lane >> 4;
#pragma unroll
  for (int rb = 0; rb < 2; ++rb) {
    const int row = m0 + 32 * w + 16 * rb + (lane & 15);
    u16* hr = hbuf + (size_t)row * F_ + n0 + 4 * g;
#pragma unroll
    for (int c = 0; c < 9; ++c) {
      float hv[4];
#pragma unroll
      for (int i = 0; i < 4; ++i) {
        const float gt = bfr(acc[rb][c][i]), up = bfr(acc[rb][c + 9][i]);
        const float sl = bfr(gt / (1.0f + __expf(-gt)));
        hv[i] = sl * up;
      }
      u32x2 o = {pk2(hv[0], hv[1]), pk2(hv[2], hv[3])};
      *(u32x2*)(hr + 16 * c) = o;
    }
#pragma unroll
    for (int c = 0; c < 4; ++c) {
      const int t = qt0 + c;
      if (t < qt1) {
#pragma unroll
        for (int i = 0; i < 4; ++i) {
          const int col = 16 * t + 4 * g + i;
          const int which = col / D_, cc = col - which * D_, head = cc / DH_, d = cc - head * DH_;
          const float v = acc[rb][18 + c][i];
          if (which == 0) {
            qbuf[((size_t)head * S_ + row) * 32 + d] = f2bf(v * QSCALE);
          } else if (which == 1) {
            if (d < 16) kmain[(((size_t)head * 64 + (row >> 5)) * 2 + (d >> 3)) * 256 + (row & 31) * 8 + (d & 7)] = f2bf(v);
            else ktail[((size_t)head * S_ + row) * 2 + (d - 16)] = f2bf(v);
          } else {
            const int kk = row & 15, hh = (kk >> 2) & 1, j = (kk & 3) | ((kk >> 3) << 2);
            vbuf[((((size_t)head * 128 + (row >> 4)) * 2 + hh) * DH_ + d) * 8 + j] = f2bf(v);
          }
        }
      }
    }
  }
}

constexpr int AT_KM = 0, AT_KT = 65536, AT_V = 65536 + 8192, AT_VSTEP = 608, AT_LDS = AT_V + 128 * AT_VSTEP;
constexpr float AT_THR = 8.0f;

DI float max3f(float a, float b, float c) { return fmaxf(fmaxf(a, b), c); }

__global__ __launch_bounds__(512, 2) void k_attn(const u16* __restrict__ qbuf, const u16* __restrict__ kmain,
                                                 const u16* __restrict__ ktail, const u16* __restrict__ vbuf,
                                                 u16* __restrict__ obuf) {
  extern __shared__ __attribute__((aligned(16))) char smem[];
  const int tid = threadIdx.x, lane = tid & 63, w = tid >> 6, r = lane & 31, hf = lane >> 5;
  const int head = blockIdx.x >> 3, qb = blockIdx.x & 7;
  {
    const u32x4* km = (const u32x4*)(kmain + (size_t)head * S_ * 16);
#pragma unroll
    for (int i = 0; i < 8; ++i) *(u32x4*)(smem + AT_KM + (i * 512 + tid) * 16) = km[i * 512 + tid];
    const u32x4* kt = (const u32x4*)(ktail + (size_t)head * S_ * 2);
    *(u32x4*)(smem + AT_KT + tid * 16) = kt[tid];
    const u32x4* vv = (const u32x4*)(vbuf + (size_t)head * S_ * DH_);
#pragma unroll
    for (int i = 0; i < 9; ++i) {
      const int p = i * 512 + tid;
      const int sh = p / DH_, d = p - sh * DH_;
      *(u32x4*)(smem + AT_V + sh * 304 + d * 16) = vv[p];
    }
    if (tid < 256) {
      const u32x4 ones = {0x3f803f80u, 0x3f803f80u, 0x3f803f80u, 0x3f803f80u};
      *(u32x4*)(smem + AT_V + tid * 304 + DH_ * 16) = ones;
    }
  }
  const int qrow = qb * 256 + w * 32 + r;
  const u16* qp = qbuf + ((size_t)head * S_ + qrow) * 32;
  const s16x8 q0 = *(const s16x8*)(qp + 8 * hf);
  u32x4 q1u = {0u, 0u, 0u, 0u};
  if (hf == 0) q1u[0] = *(const unsigned*)(qp + 16);
  const s16x8 q1 = __builtin_bit_cast(s16x8, q1u);
  __syncthreads();

  const char* kmp = smem + AT_KM + hf * 512 + r * 16;
  const char* ktp = smem + AT_KT + r * 4;
  const char* vp = smem + AT_V + hf * 304 + (r < DH_ ? r : DH_) * 16;

  f32x16 o;
  f32x16 cm;
#pragma unroll
  for (int i = 0; i < 16; ++i) { o[i] = 0.f; cm[i] = 0.f; }
  float mrun = 0.f;

  for (int t = 0; t < 64; ++t) {
    const s16x8 kf0 = *(const s16x8*)(kmp + t * 1024);
    u32x4 k1u = {*(const unsigned*)(ktp + t * 128), 0u, 0u, 0u};
    const s16x8 kf1 = __builtin_bit_cast(s16x8, k1u);
    const s16x8 vf0 = *(const s16x8*)(vp + (2 * t) * AT_VSTEP);
    const s16x8 vf1 = *(const s16x8*)(vp + (2 * t + 1) * AT_VSTEP);
    f32x16 s = __builtin_amdgcn_mfma_f32_32x32x16_bf16(kf0, q0, cm, 0, 0, 0);
    s = __builtin_amdgcn_mfma_f32_32x32x16_bf16(kf1, q1, s, 0, 0, 0);
    float mx = max3f(s[0], s[1], s[2]);
#pragma unroll
    for (int i = 3; i < 15; i += 2) mx = max3f(mx, s[i], s[i + 1]);
    mx = fmaxf(mx, s[15]);
    if (t == 0 || __any(mx > AT_THR)) {
      float mq = fmaxf(mx, __shfl_xor(mx, 32));
      if (t != 0) mq = fmaxf(mq, 0.f);
      const float al = __builtin_amdgcn_exp2f(-mq);
      mrun += mq;
#pragma unroll
      for (int i = 0; i < 16; ++i) { s[i] -= mq; o[i] *= al; cm[i] = -mrun; }
    }
    u32x4 p0, p1;
#pragma unroll
    for (int i = 0; i < 4; ++i) {
      p0[i] = pk2(__builtin_amdgcn_exp2f(s[2 * i]), __builtin_amdgcn_exp2f(s[2 * i + 1]));
      p1[i] = pk2(__builtin_amdgcn_exp2f(s[8 + 2 * i]), __builtin_amdgcn_exp2f(s[8 + 2 * i + 1]));
    }
    o = __builtin_amdgcn_mfma_f32_32x32x16_bf16(vf0, __builtin_bit_cast(s16x8, p0), o, 0, 0, 0);
    o = __builtin_amdgcn_mfma_f32_32x32x16_bf16(vf1, __builtin_bit_cast(s16x8, p1), o, 0, 0, 0);
  }
  float l = o[10];
  l = __shfl(l, r);
  const float inv = 1.0f / l;
  u16* op = obuf + (size_t)qrow * D_ + head * DH_ + 4 * hf;
  *(unsigned*)(op + 0) = pk2(o[0] * inv, o[1] * inv);
  *(unsigned*)(op + 2) = pk2(o[2] * inv, o[3] * inv);
  *(unsigned*)(op + 8) = pk2(o[4] * inv, o[5] * inv);
  *(unsigned*)(op + 10) = pk2(o[6] * inv, o[7] * inv);
  if (hf == 0) *(unsigned*)(op + 16) = pk2(o[8] * inv, o[9] * inv);
}

constexpr int DSPLIT = 4;
typedef Gemm<4, 9, 2, 3> G2;
__global__ __launch_bounds__(256, 2) void k_down(const u16* __restrict__ hbuf, const u16* __restrict__ w2,
                                                 float* __restrict__ part) {
  extern __shared__ __attribute__((aligned(16))) char smem[];
  const int b = blockIdx.x;
  const int split = b & 3, cg = (b >> 2) & 3, panel = b >> 4;
  const int m0 = panel * 128, n0 = cg * 144, k0 = split * (F_ / DSPLIT);
  auto colptr = [&](int c, int& ld) -> const u16* { ld = D_; return w2 + (size_t)k0 * D_ + n0 + 16 * c; };
  f32x4 acc[2][9];
  G2::run(smem, hbuf + (size_t)m0 * F_ + k0, F_, (F_ / DSPLIT) / 64, colptr, acc);
  const int lane = threadIdx.x & 63, w = threadIdx.x >> 6, g = lane >> 4;
#pragma unroll
  for (int rb = 0; rb < 2; ++rb) {
    const int row = m0 + 32 * w + 16 * rb + (lane & 15);
    float* pr = part + ((size_t)split * S_ + row) * D_ + n0 + 4 * g;
#pragma unroll
    for (int c = 0; c < 9; ++c) *(f32x4*)(pr + 16 * c) = acc[rb][c];
  }
}

typedef Gemm<4, 3, 2, 3> G3;
__global__ __launch_bounds__(256, 2) void k_final(const u16* __restrict__ obuf, const u16* __restrict__ wo,
                                                  const float* __restrict__ part, const u16* __restrict__ x,
                                                  u16* __restrict__ out) {
  extern __shared__ __attribute__((aligned(16))) char smem[];
  const int b = blockIdx.x;
  const int cg = b % 12, panel = b / 12;
  const int m0 = panel * 128, n0 = cg * 48;
  auto colptr = [&](int c, int& ld) -> const u16* { ld = D_; return wo + n0 + 16 * c; };
  f32x4 acc[2][3];
  G3::run(smem, obuf + (size_t)m0 * D_, D_, D_ / 64, colptr, acc);
  const int lane = threadIdx.x & 63, w = threadIdx.x >> 6, g = lane >> 4;
#pragma unroll
  for (int rb = 0; rb < 2; ++rb) {
    const int row = m0 + 32 * w + 16 * rb + (lane & 15);
#pragma unroll
    for (int c = 0; c < 3; ++c) {
      const int col = n0 + 16 * c + 4 * g;
      f32x4 f = {0.f, 0.f, 0.f, 0.f};
#pragma unroll
      for (int sp = 0; sp < DSPLIT; ++sp) f += *(const f32x4*)(part + ((size_t)sp * S_ + row) * D_ + col);
      const u32x2 xv = *(const u32x2*)(x + (size_t)row * D_ + col);
      float xf[4] = {__uint_as_float(xv[0] << 16), __uint_as_float(xv[0] & 0xffff0000u),
                     __uint_as_float(xv[1] << 16), __uint_as_float(xv[1] & 0xffff0000u)};
      float r4[4];
#pragma unroll
      for (int i = 0; i < 4; ++i) r4[i] = bfr(xf[i] + bfr(acc[rb][c][i])) + bfr(f[i]);
      u32x2 o = {pk2(r4[0], r4[1]), pk2(r4[2], r4[3])};
      *(u32x2*)(out + (size_t)row * D_ + col) = o;
    }
  }
}

constexpr size_t WS_XN = 0;
constexpr size_t WS_H = WS_XN + (size_t)S_ * D_ * 2;
constexpr size_t WS_Q = WS_H + (size_t)S_ * F_ * 2;
constexpr size_t WS_KM = WS_Q + (size_t)H_ * S_ * 32 * 2;
constexpr size_t WS_KT = WS_KM + (size_t)H_ * S_ * 16 * 2;
constexpr size_t WS_V = WS_KT + (size_t)H_ * S_ * 2 * 2;
constexpr size_t WS_O = WS_V + (size_t)H_ * S_ * DH_ * 2;
constexpr size_t WS_P = WS_O + (size_t)S_ * D_ * 2;
constexpr size_t WS_END = WS_P + (size_t)DSPLIT * S_ * D_ * 4;

extern "C" void kernel_launch(void* const* d_in, const int* in_sizes, int n_in, void* d_out, int out_size,
                              void* d_ws, size_t ws_size, hipStream_t stream) {
  const u16* x = (const u16*)d_in[0];
  const u16* wqkv = (const u16*)d_in[1];
  const u16* wo = (const u16*)d_in[2];
  const u16* w1 = (const u16*)d_in[4];
  const u16* w3 = (const u16*)d_in[5];
  const u16* w2 = (const u16*)d_in[6];
  char* ws = (char*)d_ws;
  u16* xn = (u16*)(ws + WS_XN);
  u16* hbuf = (u16*)(ws + WS_H);
  u16* qbuf = (u16*)(ws + WS_Q);
  u16* kmain = (u16*)(ws + WS_KM);
  u16* ktail = (u16*)(ws + WS_KT);
  u16* vbuf = (u16*)(ws + WS_V);
  u16* obuf = (u16*)(ws + WS_O);
  float* part = (float*)(ws + WS_P);
  u16* out = (u16*)d_out;

  hipFuncSetAttribute((const void*)k_gemm1, hipFuncAttributeMaxDynamicSharedMemorySize, G1::STAGE * 3);
  hipFuncSetAttribute((const void*)k_attn, hipFuncAttributeMaxDynamicSharedMemorySize, AT_LDS);
  hipFuncSetAttribute((const void*)k_down, hipFuncAttributeMaxDynamicSharedMemorySize, G2::STAGE * 3);
  hipFuncSetAttribute((const void*)k_final, hipFuncAttributeMaxDynamicSharedMemorySize, G3::STAGE * 3);

  k_ln<<<S_ / 4, 256, 0, stream>>>(x, xn);
  k_gemm1<<<256, 512, G1::STAGE * 3, stream>>>(xn, wqkv, w1, w3, hbuf, qbuf, kmain, ktail, vbuf);
  k_attn<<<256, 512, AT_LDS, stream>>>(qbuf, kmain, ktail, vbuf, obuf);
  k_down<<<256, 256, G2::STAGE * 3, stream>>>(hbuf, w2, part);
  k_final<<<192, 256, G3::STAGE * 3, stream>>>(obuf, wo, part, x, out);
}
```

```cpp
#include <hip/hip_runtime.h>
#include <hip/hip_bf16.h>
#include <stdint.h>

typedef __attribute__((ext_vector_type(4))) short s16x4;
typedef __attribute__((ext_vector_type(8))) short s16x8;
typedef __attribute__((ext_vector_type(4))) float f32x4;
typedef __attribute__((ext_vector_type(16))) float f32x16;
typedef __attribute__((ext_vector_type(2))) unsigned u32x2;
typedef __attribute__((ext_vector_type(4))) unsigned u32x4;
typedef __attribute__((ext_vector_type(2))) __bf16 bf16x2;
typedef unsigned short u16;
typedef __attribute__((address_space(3))) s16x4* lds_s16x4_ptr;
typedef __attribute__((address_space(3))) char* lds_char_ptr;

constexpr int S_ = 2048, D_ = 576, H_ = 32, DH_ = 18, F_ = 4608, QKV_ = 1728;

#define DI __device__ __forceinline__

DI float bf2f(u16 v) { return __uint_as_float(((unsigned)v) << 16); }
DI u16 f2bf(float f) { return __builtin_bit_cast(u16, (__bf16)f); }
DI float bfr(float f) { return bf2f(f2bf(f)); }
DI unsigned pk2(float a, float b) { bf16x2 v = {(__bf16)a, (__bf16)b}; return __builtin_bit_cast(unsigned, v); }

DI void glds16(const void* gsrc, unsigned lds_dst) {
  unsigned keep;
  asm volatile("s_mov_b32 %0, m0\n\ts_mov_b32 m0, %2\n\ts_nop 0\n\tglobal_load_lds_dwordx4 %1, off\n\ts_mov_b32 m0, %0"
               : "=&s"(keep) : "v"(gsrc), "s"(lds_dst) : "memory");
}
template <int N> DI void wait_vm_bar() { asm volatile("s_waitcnt vmcnt(%0)\n\ts_barrier" ::"i"(N) : "memory"); }

DI unsigned lds_base(char* smem) { return (unsigned)(uintptr_t)(lds_char_ptr)smem; }

__global__ __launch_bounds__(256) void k_ln(const u16* __restrict__ x, u16* __restrict__ xn) {
  const int row = blockIdx.x * 4 + (threadIdx.x >> 6), lane = threadIdx.x & 63;
  const u16* xr = x + (size_t)row * D_;
  u32x4 c0 = *(const u32x4*)(xr + lane * 8);
  u32x4 c1 = {0u, 0u, 0u, 0u};
  const bool two = lane < 8;
  if (two) c1 = *(const u32x4*)(xr + 512 + lane * 8);
  float v[16];
#pragma unroll
  for (int i = 0; i < 4; ++i) {
    v[2 * i] = __uint_as_float(c0[i] << 16); v[2 * i + 1] = __uint_as_float(c0[i] & 0xffff0000u);
    v[8 + 2 * i] = __uint_as_float(c1[i] << 16); v[8 + 2 * i + 1] = __uint_as_float(c1[i] & 0xffff0000u);
  }
  float s = 0.f;
#pragma unroll
  for (int i = 0; i < 16; ++i) s += v[i];
#pragma unroll
  for (int o = 32; o > 0; o >>= 1) s += __shfl_xor(s, o);
  const float mean = s * (1.0f / D_);
  float q = 0.f;
#pragma unroll
  for (int i = 0; i < 8; ++i) { float d = v[i] - mean; q += d * d; }
  if (two) {
#pragma unroll
    for (int i = 8; i < 16; ++i) { float d = v[i] - mean; q += d * d; }
  }
#pragma unroll
  for (int o = 32; o > 0; o >>= 1) q += __shfl_xor(q, o);
  const float var = bfr(q * (1.0f / D_));
  const float m = bfr(mean);
  const float sd = bfr(sqrtf(bfr(var + 1e-5f)));
  u32x4 o0, o1;
#pragma unroll
  for (int i = 0; i < 4; ++i) {
    o0[i] = pk2(bfr(v[2 * i] - m) / sd, bfr(v[2 * i + 1] - m) / sd);
    o1[i] = pk2(bfr(v[8 + 2 * i] - m) / sd, bfr(v[8 + 2 * i + 1] - m) / sd);
  }
  u16* orow = xn + (size_t)row * D_;
  *(u32x4*)(orow + lane * 8) = o0;
  if (two) *(u32x4*)(orow + 512 + lane * 8) = o1;
}

template <int WR, int WC, int RB, int NTW, int KS, int NSTAGE>
struct Gemm {
  static constexpr int NW = WR * WC;
  static constexpr int ROWS = WR * RB * 16;
  static constexpr int NT = WC * NTW;
  static constexpr int NG = (NT + 3) / 4;
  static constexpr int BSTEP = 4 * NG * 1024;
  static constexpr int ASTEP = ROWS * 64;
  static constexpr int STAGE = KS * (BSTEP + ASTEP);
  static constexpr int LDS = STAGE * NSTAGE;
  static constexpr int NBT = KS * 4 * NG;
  static constexpr int NAT = KS * (ROWS / 16);
  static constexpr int NI = NBT + NAT;
  static constexpr int NGL = (NI + NW - 1) / NW;

  template <typename ColFn>
  static DI void run(char* smem, const u16* __restrict__ A, int lda, int nstages, ColFn colptr, f32x4 (&acc)[RB][NTW]) {
    const int tid = threadIdx.x, lane = tid & 63;
    const int w = __builtin_amdgcn_readfirstlane(tid >> 6);
    const int wr = w / WC, wc = w % WC;
    const unsigned lds0 = lds_base(smem);
    const u16* src[NGL]; int sstep[NGL]; unsigned sdst[NGL];
#pragma unroll
    for (int j = 0; j < NGL; ++j) {
      int id = w + NW * j;
      if (id >= NI) id -= NI;
      if (id < NBT) {
        const int ks = id / (4 * NG), r1 = id % (4 * NG), kb = r1 / NG, grp = r1 % NG;
        const int row = lane >> 3, ip = (lane & 7) >> 1, half = lane & 1;
        const int swz = ((row >> 1) & 1) | ((kb & 1) << 1);
        int c = grp * 4 + (ip ^ swz);
        c = c < NT ? c : NT - 1;
        int ld;
        const u16* p = colptr(c, ld);
        src[j] = p + (size_t)(ks * 32 + kb * 8 + row) * ld + 8 * half;
        sstep[j] = KS * 32 * ld;
        sdst[j] = (unsigned)id * 1024u;
      } else {
        const int ia = id - NBT, ks = ia / (ROWS / 16), rb = ia % (ROWS / 16);
        const int r = 16 * rb + (lane >> 2), cp = lane & 3;
        const int ch = cp ^ ((-(r >> 2)) & 3);
        src[j] = A + (size_t)r * lda + ks * 32 + ch * 8;
        sstep[j] = KS * 32;
        sdst[j] = (unsigned)(KS * BSTEP + ks * ASTEP + rb * 1024);
      }
    }
    unsigned ibuf = 0;
    auto issue = [&]() {
#pragma unroll
      for (int j = 0; j < NGL; ++j) {
        glds16(src[j], lds0 + ibuf + sdst[j]);
        src[j] += sstep[j];
      }
      ibuf = ibuf + STAGE == NSTAGE * STAGE ? 0u : ibuf + STAGE;
    };
    const int g = lane >> 4, q = (lane & 15) >> 2, p4 = lane & 3;
    const int bswz = ((q >> 1) & 1) | ((g & 1) << 1);
    int boff[4];
#pragma unroll
    for (int i = 0; i < 4; ++i) boff[i] = g * NG * 1024 + q * 128 + ((i ^ bswz) * 32) + p4 * 8;
    const int ar = lane & 15;
    const int aoff = KS * BSTEP + wr * RB * 1024 + ar * 64 + ((g ^ ((-(ar >> 2)) & 3)) * 16);

#pragma unroll
    for (int rb = 0; rb < RB; ++rb)
#pragma unroll
      for (int c = 0; c < NTW; ++c) acc[rb][c] = f32x4{0.f, 0.f, 0.f, 0.f};

#pragma unroll
    for (int s = 0; s < NSTAGE - 1; ++s) if (s < nstages) issue();

    auto ldb = [&](const char* sbk, int c) -> s16x8 {
      const char* bp = sbk + boff[c & 3] + (c >> 2) * 1024;
      const s16x4 b0 = __builtin_amdgcn_ds_read_tr16_b64_v4i16((lds_s16x4_ptr)(bp));
      const s16x4 b1 = __builtin_amdgcn_ds_read_tr16_b64_v4i16((lds_s16x4_ptr)(bp + 512));
      return __builtin_shufflevector(b0, b1, 0, 1, 2, 3, 4, 5, 6, 7);
    };

    int bo[4];
#pragma unroll
    for (int i = 0; i < 4; ++i) {
      const int cg = wc * NTW + i;
      bo[i] = ((cg & 3) == 0 ? boff[0] : (cg & 3) == 1 ? boff[1] : (cg & 3) == 2 ? boff[2] : boff[3]) + (cg >> 2) * 1024;
    }
    auto ldbw = [&](const char* sbk, int c) -> s16x8 {
      const char* bp = sbk + bo[c & 3] + (c >> 2) * 1024;
      const s16x4 b0 = __builtin_amdgcn_ds_read_tr16_b64_v4i16((lds_s16x4_ptr)(bp));
      const s16x4 b1 = __builtin_amdgcn_ds_read_tr16_b64_v4i16((lds_s16x4_ptr)(bp + 512));
      return __builtin_shufflevector(b0, b1, 0, 1, 2, 3, 4, 5, 6, 7);
    };
    (void)ldb;

    unsigned cbuf = 0;
#pragma unroll 1
    for (int s = 0; s < nstages; ++s) {
      if (s + NSTAGE - 2 < nstages) wait_vm_bar<(NSTAGE - 2) * NGL>(); else wait_vm_bar<0>();
      if (s + NSTAGE - 1 < nstages) issue();
      const char* sb = smem + cbuf;
      cbuf = cbuf + STAGE == NSTAGE * STAGE ? 0u : cbuf + STAGE;
#pragma unroll
      for (int ks = 0; ks < KS; ++ks) {
        const char* sbk = sb + ks * BSTEP;
        s16x8 a[RB];
        s16x8 bq[3];
        bq[0] = ldbw(sbk, 0);
#pragma unroll
        for (int rb = 0; rb < RB; ++rb) a[rb] = *(const s16x8*)(sb + aoff + ks * ASTEP + rb * 1024);
        if (NTW > 1) bq[1] = ldbw(sbk, 1);
#pragma unroll
        for (int c = 0; c < NTW; ++c) {
          if (c + 2 < NTW) bq[(c + 2) % 3] = ldbw(sbk, c + 2);
#pragma unroll
          for (int rb = 0; rb < RB; ++rb)
            acc[rb][c] = __builtin_amdgcn_mfma_f32_16x16x32_bf16(bq[c % 3], a[rb], acc[rb][c], 0, 0, 0);
          __builtin_amdgcn_sched_group_barrier(0x100, 2, 0);
          __builtin_amdgcn_sched_group_barrier(0x008, RB, 0);
        }
      }
    }
  }
};

typedef Gemm<4, 2, 4, 11, 1, 3> G1;
constexpr float QSCALE = 0.23570226039551584f * 1.4426950408889634f;
constexpr int KHS = 64 * 768;
constexpr int VHS = 128 * 288;

DI float silu_mul(float gacc, float uacc) {
  const float gt = bfr(gacc), up = bfr(uacc);
  const float e = __builtin_amdgcn_exp2f(gt * -1.4426950408889634f);
  const float sl = bfr(gt * __builtin_amdgcn_rcpf(1.0f + e));
  return sl * up;
}

__global__ __launch_bounds__(512, 2) void k_gemm1(const u16* __restrict__ xn, const u16* __restrict__ wqkv,
                                                  const u16* __restrict__ w1, const u16* __restrict__ w3,
                                                  u16* __restrict__ hbuf, u16* __restrict__ qbuf,
                                                  u16* __restrict__ kbuf, u16* __restrict__ vbuf) {
  extern __shared__ __attribute__((aligned(16))) char smem[];
  const int b = blockIdx.x;
  const int cgp = (b & 7) * 4 + ((b >> 3) & 3);
  const int panel = b >> 5;
  const int m0 = panel * 256, n0 = cgp * 144;
  const int qt0 = (108 * cgp) >> 5, qt1 = (108 * (cgp + 1)) >> 5;
  auto colptr = [&](int c, int& ld) -> const u16* {
    int t;
    if (c < 11) {
      if (c < 5) { ld = F_; return w1 + n0 + 16 * c; }
      if (c < 10) { ld = F_; return w3 + n0 + 16 * (c - 5); }
      t = qt0;
    } else {
      const int d = c - 11;
      if (d < 4) { ld = F_; return w1 + n0 + 16 * (5 + d); }
      if (d < 8) { ld = F_; return w3 + n0 + 16 * (1 + d); }
      t = qt0 + 1 + (d - 8);
    }
    t = t < qt1 ? t : qt1 - 1;
    ld = QKV_; return wqkv + 16 * t;
  };
  f32x4 acc[4][11];
  G1::run(smem, xn + (size_t)m0 * D_, D_, D_ / 32, colptr, acc);

  const int lane = threadIdx.x & 63, g = lane >> 4;
  const int w = __builtin_amdgcn_readfirstlane(threadIdx.x >> 6);
  const int wr = w >> 1, wc = w & 1;
  const int row0 = m0 + 64 * wr + (lane & 15);
  {
    u16* hr = hbuf + (size_t)row0 * F_ + n0 + 4 * g + (wc ? 80 : 0);
    if (wc == 0) {
#pragma unroll
      for (int rb = 0; rb < 4; ++rb)
#pragma unroll
        for (int c = 0; c < 5; ++c) {
          u32x2 o = {pk2(silu_mul(acc[rb][c][0], acc[rb][c + 5][0]), silu_mul(acc[rb][c][1], acc[rb][c + 5][1])),
                     pk2(silu_mul(acc[rb][c][2], acc[rb][c + 5][2]), silu_mul(acc[rb][c][3], acc[rb][c + 5][3]))};
          *(u32x2*)(hr + (size_t)rb * 16 * F_ + 16 * c) = o;
        }
    } else {
#pragma unroll
      for (int rb = 0; rb < 4; ++rb)
#pragma unroll
        for (int c = 0; c < 4; ++c) {
          u32x2 o = {pk2(silu_mul(acc[rb][c][0], acc[rb][c + 4][0]), silu_mul(acc[rb][c][1], acc[rb][c + 4][1])),
                     pk2(silu_mul(acc[rb][c][2], acc[rb][c + 4][2]), silu_mul(acc[rb][c][3], acc[rb][c + 4][3]))};
          *(u32x2*)(hr + (size_t)rb * 16 * F_ + 16 * c) = o;
        }
    }
  }
#pragma unroll
  for (int c = 8; c < 11; ++c) {
    int t;
    if (wc == 0) { if (c != 10) continue; t = qt0; } else t = qt0 + 1 + (c - 8);
    if (t >= qt1) continue;
    const int which = t / 36;
    const int cc0 = 16 * (t - 36 * which) + 4 * g;
#pragma unroll
    for (int i = 0; i < 4; ++i) {
      const int cc = cc0 + i, head = cc / DH_, d = cc - head * DH_;
      if (which == 0) {
        u16* p = qbuf + (size_t)head * (S_ * 32) + row0 * 32 + d;
#pragma unroll
        for (int rb = 0; rb < 4; ++rb) p[rb * 16 * 32] = f2bf(acc[rb][c][i] * QSCALE);
      } else if (which == 1) {
        u16* p = kbuf + (size_t)head * KHS + (d >> 3) * 256 + (d & 7);
#pragma unroll
        for (int rb = 0; rb < 4; ++rb) {
          const int row = row0 + 16 * rb;
          p[(row >> 5) * 768 + (row & 31) * 8] = f2bf(acc[rb][c][i]);
        }
      } else {
        const int kk = row0 & 15, hh = (kk >> 2) & 1, j = (kk & 3) | ((kk >> 3) << 2);
        u16* p = vbuf + (size_t)head * VHS + (row0 >> 4) * 288 + hh * 144 + d * 8 + j;
#pragma unroll
        for (int rb = 0; rb < 4; ++rb) p[rb * 288] = f2bf(acc[rb][c][i]);
      }
    }
  }
}

constexpr int AT_KM = 0, AT_KT = 65536, AT_V = 65536 + 16384, AT_VSTEP = 608, AT_LDS = AT_V + 128 * AT_VSTEP;
constexpr float AT_THR = 8.0f;

DI float max3f(float a, float b, float c) { return fmaxf(fmaxf(a, b), c); }
DI float rowmax16(const f32x16& s) {
  float mx = max3f(s[0], s[1], s[2]);
#pragma unroll
  for (int i = 3; i < 15; i += 2) mx = max3f(mx, s[i], s[i + 1]);
  return fmaxf(mx, s[15]);
}

__global__ __launch_bounds__(512, 2) void k_attn(const u16* __restrict__ qbuf, const u16* __restrict__ kbuf,
                                                 const u16* __restrict__ vbuf, u16* __restrict__ obuf) {
  extern __shared__ __attribute__((aligned(16))) char smem[];
  const int tid = threadIdx.x, lane = tid & 63, w = tid >> 6, r = lane & 31, hf = lane >> 5;
  const int head = blockIdx.x >> 3, qb = blockIdx.x & 7;
  {
    const u32x4* kg = (const u32x4*)(kbuf + (size_t)head * KHS);
#pragma unroll
    for (int i = 0; i < 12; ++i) {
      const int p = i * 512 + tid;
      const int tile = p / 96, rem = p - tile * 96;
      const u32x4 v = kg[p];
      if (rem < 64) *(u32x4*)(smem + AT_KM + tile * 1024 + rem * 16) = v;
      else { u32x2 t2 = {v[0], 0x3f803f80u}; *(u32x2*)(smem + AT_KT + (tile * 32 + rem - 64) * 8) = t2; }
    }
    const u32x4* vv = (const u32x4*)(vbuf + (size_t)head * VHS);
#pragma unroll
    for (int i = 0; i < 9; ++i) {
      const int p = i * 512 + tid;
      const int sh = p / DH_, d = p - sh * DH_;
      *(u32x4*)(smem + AT_V + sh * 304 + d * 16) = vv[p];
    }
    if (tid < 256) {
      const u32x4 ones = {0x3f803f80u, 0x3f803f80u, 0x3f803f80u, 0x3f803f80u};
      *(u32x4*)(smem + AT_V + tid * 304 + DH_ * 16) = ones;
    }
  }
  const int qrow = qb * 256 + w * 32 + r;
  const u16* qp = qbuf + ((size_t)head * S_ + qrow) * 32;
  const s16x8 q0 = *(const s16x8*)(qp + 8 * hf);
  u32x4 q1u = {0u, 0u, 0u, 0u};
  if (hf == 0) q1u[0] = *(const unsigned*)(qp + 16);
  __syncthreads();

  const char* kmp = smem + AT_KM + hf * 512 + r * 16;
  const char* ktp = smem + AT_KT + r * 8;
  const char* vp = smem + AT_V + hf * 304 + (r < DH_ ? r : DH_) * 16;
  const f32x16 zero16 = {0.f, 0.f, 0.f, 0.f, 0.f, 0.f, 0.f, 0.f, 0.f, 0.f, 0.f, 0.f, 0.f, 0.f, 0.f, 0.f};

  auto qk = [&](int t) -> f32x16 {
    const s16x8 kf0 = *(const s16x8*)(kmp + t * 1024);
    const u32x2 kt = *(const u32x2*)(ktp + t * 256);
    const u32x4 k1u = {kt[0], kt[1], 0u, 0u};
    f32x16 s = __builtin_amdgcn_mfma_f32_32x32x16_bf16(kf0, q0, zero16, 0, 0, 0);
    return __builtin_amdgcn_mfma_f32_32x32x16_bf16(__builtin_bit_cast(s16x8, k1u), __builtin_bit_cast(s16x8, q1u), s, 0, 0, 0);
  };

  f32x16 o = zero16;
  float mrun = 0.f;
  auto set_m = [&](float m) {
    const float mh = bfr(m), ml = bfr(m - mh);
    mrun = mh + ml;
    if (hf == 0) q1u[1] = pk2(-mh, -ml);
  };

  f32x16 sc = qk(0);
  {
    const float mx = rowmax16(sc);
    const float mq = fmaxf(mx, __shfl_xor(mx, 32));
    set_m(mq);
#pragma unroll
    for (int i = 0; i < 16; ++i) sc[i] -= mrun;
  }
#pragma unroll 2
  for (int t = 0; t < 64; ++t) {
    f32x16 sn;
    if (t + 1 < 64) sn = qk(t + 1);
    const s16x8 vf0 = *(const s16x8*)(vp + (2 * t) * AT_VSTEP);
    const s16x8 vf1 = *(const s16x8*)(vp + (2 * t + 1) * AT_VSTEP);
    u32x4 p0, p1;
#pragma unroll
    for (int i = 0; i < 4; ++i) p0[i] = pk2(__builtin_amdgcn_exp2f(sc[2 * i]), __builtin_amdgcn_exp2f(sc[2 * i + 1]));
    o = __builtin_amdgcn_mfma_f32_32x32x16_bf16(vf0, __builtin_bit_cast(s16x8, p0), o, 0, 0, 0);
#pragma unroll
    for (int i = 0; i < 4; ++i) p1[i] = pk2(__builtin_amdgcn_exp2f(sc[8 + 2 * i]), __builtin_amdgcn_exp2f(sc[8 + 2 * i + 1]));
    o = __builtin_amdgcn_mfma_f32_32x32x16_bf16(vf1, __builtin_bit_cast(s16x8, p1), o, 0, 0, 0);
    if (t + 1 < 64) {
      const float mx = rowmax16(sn);
      if (__any(mx > AT_THR)) {
        const float mq = fmaxf(fmaxf(mx, __shfl_xor(mx, 32)), 0.f);
        const float mold = mrun;
        set_m(mold + mq);
        const float dl = mrun - mold;
        const float al = __builtin_amdgcn_exp2f(-dl);
#pragma unroll
        for (int i = 0; i < 16; ++i) { sn[i] -= dl; o[i] *= al; }
      }
      sc = sn;
    }
  }
  float l = o[10];
  l = __shfl(l, r);
  const float inv = 1.0f / l;
  u16* op = obuf + (size_t)qrow * D_ + head * DH_ + 4 * hf;
  *(unsigned*)(op + 0) = pk2(o[0] * inv, o[1] * inv);
  *(unsigned*)(op + 2) = pk2(o[2] * inv, o[3] * inv);
  *(unsigned*)(op + 8) = pk2(o[4] * inv, o[5] * inv);
  *(unsigned*)(op + 10) = pk2(o[6] * inv, o[7] * inv);
  if (hf == 0) *(unsigned*)(op + 16) = pk2(o[8] * inv, o[9] * inv);
}

constexpr int DSPLIT = 5;
typedef Gemm<2, 4, 4, 3, 2, 2> G2;
__global__ __launch_bounds__(512, 2) void k_down(const u16* __restrict__ hbuf, const u16* __restrict__ w2,
                                                 float* __restrict__ part) {
  extern __shared__ __attribute__((aligned(16))) char smem[];
  const int b = blockIdx.x;
  const int split = b % DSPLIT, r1 = b / DSPLIT, cg = r1 % 3, panel = r1 / 3;
  const int c0 = (72 * split) / DSPLIT, c1 = (72 * (split + 1)) / DSPLIT;
  const int m0 = panel * 128, n0 = cg * 192, k0 = c0 * 64;
  auto colptr = [&](int c, int& ld) -> const u16* { ld = D_; return w2 + (size_t)k0 * D_ + n0 + 16 * c; };
  f32x4 acc[4][3];
  G2::run(smem, hbuf + (size_t)m0 * F_ + k0, F_, c1 - c0, colptr, acc);
  const int lane = threadIdx.x & 63, w = threadIdx.x >> 6, g = lane >> 4, wr = w >> 2, wc = w & 3;
#pragma unroll
  for (int rb = 0; rb < 4; ++rb) {
    const int row = m0 + 64 * wr + 16 * rb + (lane & 15);
    float* pr = part + ((size_t)split * S_ + row) * D_ + n0 + 48 * wc + 4 * g;
#pragma unroll
    for (int c = 0; c < 3; ++c) *(f32x4*)(pr + 16 * c) = acc[rb][c];
  }
}

typedef Gemm<2, 2, 2, 2, 2, 3> G3;
__global__ __launch_bounds__(256, 2) void k_final(const u16* __restrict__ obuf, const u16* __restrict__ wo,
                                                  const float* __restrict__ part, const u16* __restrict__ x,
                                                  u16* __restrict__ out) {
  extern __shared__ __attribute__((aligned(16))) char smem[];
  const int b = blockIdx.x;
  const int cg = b % 9, panel = b / 9;
  const int m0 = panel * 64, n0 = cg * 64;
  const int lane = threadIdx.x & 63, w = threadIdx.x >> 6, g = lane >> 4, wr = w >> 1, wc = w & 1;
  f32x4 base[2][2];
#pragma unroll
  for (int rb = 0; rb < 2; ++rb)
#pragma unroll
    for (int c = 0; c < 2; ++c) {
      const int row = m0 + 32 * wr + 16 * rb + (lane & 15), col = n0 + 32 * wc + 16 * c + 4 * g;
      f32x4 f = {0.f, 0.f, 0.f, 0.f};
#pragma unroll
      for (int sp = 0; sp < DSPLIT; ++sp) f += *(const f32x4*)(part + ((size_t)sp * S_ + row) * D_ + col);
      base[rb][c] = f;
    }
  auto colptr = [&](int c, int& ld) -> const u16* { ld = D_; return wo + n0 + 16 * c; };
  f32x4 acc[2][2];
  G3::run(smem, obuf + (size_t)m0 * D_, D_, D_ / 64, colptr, acc);
#pragma unroll
  for (int rb = 0; rb < 2; ++rb) {
#pragma unroll
    for (int c = 0; c < 2; ++c) {
      const int row = m0 + 32 * wr + 16 * rb + (lane & 15), col = n0 + 32 * wc + 16 * c + 4 * g;
      const u32x2 xv = *(const u32x2*)(x + (size_t)row * D_ + col);
      float xf[4] = {__uint_as_float(xv[0] << 16), __uint_as_float(xv[0] & 0xffff0000u),
                     __uint_as_float(xv[1] << 16), __uint_as_float(xv[1] & 0xffff0000u)};
      float r4[4];
#pragma unroll
      for (int i = 0; i < 4; ++i) r4[i] = bfr(xf[i] + bfr(acc[rb][c][i])) + bfr(base[rb][c][i]);
      u32x2 o = {pk2(r4[0], r4[1]), pk2(r4[2], r4[3])};
      *(u32x2*)(out + (size_t)row * D_ + col) = o;
    }
  }
}

constexpr size_t WS_XN = 0;
constexpr size_t WS_H = WS_XN + (size_t)S_ * D_ * 2;
constexpr size_t WS_Q = WS_H + (size_t)S_ * F_ * 2;
constexpr size_t WS_K = WS_Q + (size_t)H_ * S_ * 32 * 2;
constexpr size_t WS_V = WS_K + (size_t)H_ * KHS * 2;
constexpr size_t WS_O = WS_V + (size_t)H_ * VHS * 2;
constexpr size_t WS_P = WS_O + (size_t)S_ * D_ * 2;
constexpr size_t WS_END = WS_P + (size_t)DSPLIT * S_ * D_ * 4;

extern "C" void kernel_launch(void* const* d_in, const int* in_sizes, int n_in, void* d_out, int out_size,
                              void* d_ws, size_t ws_size, hipStream_t stream) {
  const u16* x = (const u16*)d_in[0];
  const u16* wqkv = (const u16*)d_in[1];
  const u16* wo = (const u16*)d_in[2];
  const u16* w1 = (const u16*)d_in[4];
  const u16* w3 = (const u16*)d_in[5];
  const u16* w2 = (const u16*)d_in[6];
  char* ws = (char*)d_ws;
  u16* xn = (u16*)(ws + WS_XN);
  u16* hbuf = (u16*)(ws + WS_H);
  u16* qbuf = (u16*)(ws + WS_Q);
  u16* kbuf = (u16*)(ws + WS_K);
  u16* vbuf = (u16*)(ws + WS_V);
  u16* obuf = (u16*)(ws + WS_O);
  float* part = (float*)(ws + WS_P);
  u16* out = (u16*)d_out;

  (void)hipFuncSetAttribute((const void*)k_gemm1, hipFuncAttributeMaxDynamicSharedMemorySize, G1::LDS);
  (void)hipFuncSetAttribute((const void*)k_attn, hipFuncAttributeMaxDynamicSharedMemorySize, AT_LDS);
  (void)hipFuncSetAttribute((const void*)k_down, hipFuncAttributeMaxDynamicSharedMemorySize, G2::LDS);
  (void)hipFuncSetAttribute((const void*)k_final, hipFuncAttributeMaxDynamicSharedMemorySize, G3::LDS);

  k_ln<<<S_ / 4, 256, 0, stream>>>(x, xn);
  k_gemm1<<<256, 512, G1::LDS, stream>>>(xn, wqkv, w1, w3, hbuf, qbuf, kbuf, vbuf);
  k_attn<<<256, 512, AT_LDS, stream>>>(qbuf, kbuf, vbuf, obuf);
  k_down<<<16 * 3 * DSPLIT, 512, G2::LDS, stream>>>(hbuf, w2, part);
  k_final<<<32 * 9, 256, G3::LDS, stream>>>(obuf, wo, part, x, out);
}
```

```cpp
#include <hip/hip_runtime.h>
#include <hip/hip_bf16.h>
#include <stdint.h>

typedef __attribute__((ext_vector_type(4))) short s16x4;
typedef __attribute__((ext_vector_type(8))) short s16x8;
typedef __attribute__((ext_vector_type(4))) float f32x4;
typedef __attribute__((ext_vector_type(16))) float f32x16;
typedef __attribute__((ext_vector_type(2))) unsigned u32x2;
typedef __attribute__((ext_vector_type(4))) unsigned u32x4;
typedef __attribute__((ext_vector_type(2))) __bf16 bf16x2;
typedef unsigned short u16;
typedef __attribute__((address_space(3))) s16x4* lds_s16x4_ptr;
typedef __attribute__((address_space(3))) char* lds_char_ptr;

constexpr int S_ = 2048, D_ = 576, H_ = 32, DH_ = 18, F_ = 4608, QKV_ = 1728;

#define DI __device__ __forceinline__

DI float bf2f(u16 v) { return __uint_as_float(((unsigned)v) << 16); }
DI u16 f2bf(float f) { return __builtin_bit_cast(u16, (__bf16)f); }
DI float bfr(float f) { return bf2f(f2bf(f)); }
DI unsigned pk2(float a, float b) { bf16x2 v = {(__bf16)a, (__bf16)b}; return __builtin_bit_cast(unsigned, v); }

DI void glds16(const void* gsrc, unsigned lds_dst) {
  unsigned keep;
  asm volatile("s_mov_b32 %0, m0\n\ts_mov_b32 m0, %2\n\ts_nop 0\n\tglobal_load_lds_dwordx4 %1, off\n\ts_mov_b32 m0, %0"
               : "=&s"(keep) : "v"(gsrc), "s"(lds_dst) : "memory");
}
DI void glds16s(const void* sbase, unsigned voff, unsigned lds_dst) {
  unsigned keep;
  asm volatile("s_mov_b32 %0, m0\n\ts_mov_b32 m0, %3\n\ts_nop 0\n\tglobal_load_lds_dwordx4 %1, %2\n\ts_mov_b32 m0, %0"
               : "=&s"(keep) : "v"(voff), "s"(sbase), "s"(lds_dst) : "memory");
}
template <int N> DI void wait_vm_bar() { asm volatile("s_waitcnt vmcnt(%0)\n\ts_barrier" ::"i"(N) : "memory"); }

DI unsigned lds_base(char* smem) { return (unsigned)(uintptr_t)(lds_char_ptr)smem; }

DI size_t atile_off(int row, int k, int nks) {
  const int r = row & 15, ch = (k >> 3) & 3;
  return ((size_t)(row >> 4) * nks + (k >> 5)) * 1024 + r * 64 + ((ch ^ ((-(r >> 2)) & 3)) << 4) + (k & 7) * 2;
}

__global__ __launch_bounds__(256) void k_ln(const u16* __restrict__ x, char* __restrict__ xn) {
  const int row = blockIdx.x * 4 + (threadIdx.x >> 6), lane = threadIdx.x & 63;
  const u16* xr = x + (size_t)row * D_;
  u32x4 c0 = *(const u32x4*)(xr + lane * 8);
  u32x4 c1 = {0u, 0u, 0u, 0u};
  const bool two = lane < 8;
  if (two) c1 = *(const u32x4*)(xr + 512 + lane * 8);
  float v[16];
#pragma unroll
  for (int i = 0; i < 4; ++i) {
    v[2 * i] = __uint_as_float(c0[i] << 16); v[2 * i + 1] = __uint_as_float(c0[i] & 0xffff0000u);
    v[8 + 2 * i] = __uint_as_float(c1[i] << 16); v[8 + 2 * i + 1] = __uint_as_float(c1[i] & 0xffff0000u);
  }
  float s = 0.f;
#pragma unroll
  for (int i = 0; i < 16; ++i) s += v[i];
#pragma unroll
  for (int o = 32; o > 0; o >>= 1) s += __shfl_xor(s, o);
  const float mean = s * (1.0f / D_);
  float q = 0.f;
#pragma unroll
  for (int i = 0; i < 8; ++i) { float d = v[i] - mean; q += d * d; }
  if (two) {
#pragma unroll
    for (int i = 8; i < 16; ++i) { float d = v[i] - mean; q += d * d; }
  }
#pragma unroll
  for (int o = 32; o > 0; o >>= 1) q += __shfl_xor(q, o);
  const float var = bfr(q * (1.0f / D_));
  const float m = bfr(mean);
  const float sd = bfr(sqrtf(bfr(var + 1e-5f)));
  u32x4 o0, o1;
#pragma unroll
  for (int i = 0; i < 4; ++i) {
    o0[i] = pk2(bfr(v[2 * i] - m) / sd, bfr(v[2 * i + 1] - m) / sd);
    o1[i] = pk2(bfr(v[8 + 2 * i] - m) / sd, bfr(v[8 + 2 * i + 1] - m) / sd);
  }
  *(u32x4*)(xn + atile_off(row, lane * 8, D_ / 32)) = o0;
  if (two) *(u32x4*)(xn + atile_off(row, 512 + lane * 8, D_ / 32)) = o1;
}

template <int WR, int WC, int RB, int NTW, int NG, int KS, int NSTAGE>
struct Gemm {
  static constexpr int NW = WR * WC;
  static constexpr int ROWS = WR * RB * 16;
  static constexpr int BSTEP = 4 * NG * 1024;
  static constexpr int ASTEP = ROWS * 64;
  static constexpr int STAGE = KS * (BSTEP + ASTEP);
  static constexpr int LDS = STAGE * NSTAGE;
  static constexpr int NBT = KS * 4 * NG;
  static constexpr int NAT = KS * (ROWS / 16);
  static constexpr int NI = NBT + NAT;
  static constexpr int NGL = (NI + NW - 1) / NW;

  template <typename WinFn, typename TileFn>
  static DI void run(char* smem, const char* __restrict__ A, size_t rbs, int nstages, WinFn winptr, TileFn tile,
                     f32x4 (&acc)[RB][NTW]) {
    const int tid = threadIdx.x, lane = tid & 63;
    const int w = __builtin_amdgcn_readfirstlane(tid >> 6);
    const int wr = w / WC;
    const unsigned lds0 = lds_base(smem);
    const char* src[NGL]; int sstep[NGL]; unsigned sdst[NGL]; unsigned voff[NGL];
#pragma unroll
    for (int j = 0; j < NGL; ++j) {
      int id = w + NW * j;
      if (id >= NI) id -= NI;
      if (id < NBT) {
        const int ks = id / (4 * NG), r1 = id % (4 * NG), kb = r1 / NG, grp = r1 % NG;
        const int row = lane >> 3, ip = (lane & 7) >> 1, half = lane & 1;
        const int swz = ((row >> 1) & 1) | ((kb & 1) << 1);
        int ld;
        const u16* p = winptr(grp, ld);
        src[j] = (const char*)(p + (size_t)(ks * 32 + kb * 8) * ld);
        voff[j] = (unsigned)(row * ld + 16 * (ip ^ swz) + 8 * half) * 2u;
        sstep[j] = KS * 32 * ld * 2;
        sdst[j] = (unsigned)id * 1024u;
      } else {
        const int ia = id - NBT, ks = ia / (ROWS / 16), rb = ia % (ROWS / 16);
        src[j] = A + rb * rbs + ks * 1024;
        voff[j] = (unsigned)lane * 16u;
        sstep[j] = KS * 1024;
        sdst[j] = (unsigned)(KS * BSTEP + ks * ASTEP + rb * 1024);
      }
    }
    unsigned ibuf = 0;
    auto issue1 = [&](int j) {
      glds16s(src[j], voff[j], lds0 + ibuf + sdst[j]);
      src[j] += sstep[j];
    };
    auto issue_done = [&]() { ibuf = ibuf + STAGE == NSTAGE * STAGE ? 0u : ibuf + STAGE; };
    const int g = lane >> 4, q = (lane & 15) >> 2, p4 = lane & 3;
    const int bswz = ((q >> 1) & 1) | ((g & 1) << 1);
    int bo[NTW];
#pragma unroll
    for (int c = 0; c < NTW; ++c) {
      const int t = tile(c);
      bo[c] = g * NG * 1024 + q * 128 + (((t & 3) ^ bswz) * 32) + p4 * 8 + (t >> 2) * 1024;
    }
    const int ar = lane & 15;
    const int aoff = KS * BSTEP + wr * RB * 1024 + ar * 64 + ((g ^ ((-(ar >> 2)) & 3)) * 16);

#pragma unroll
    for (int rb = 0; rb < RB; ++rb)
#pragma unroll
      for (int c = 0; c < NTW; ++c) acc[rb][c] = f32x4{0.f, 0.f, 0.f, 0.f};

    auto ldb = [&](const char* sbk, int c) -> s16x8 {
      const char* bp = sbk + bo[c];
      const s16x4 b0 = __builtin_amdgcn_ds_read_tr16_b64_v4i16((lds_s16x4_ptr)(bp));
      const s16x4 b1 = __builtin_amdgcn_ds_read_tr16_b64_v4i16((lds_s16x4_ptr)(bp + 512));
      return __builtin_shufflevector(b0, b1, 0, 1, 2, 3, 4, 5, 6, 7);
    };

#pragma unroll
    for (int s = 0; s < NSTAGE - 1; ++s)
      if (s < nstages) {
#pragma unroll
        for (int j = 0; j < NGL; ++j) issue1(j);
        issue_done();
      }
    if (nstages > 1) wait_vm_bar<(NSTAGE - 3 > 0 ? NSTAGE - 3 : 0) * NGL>(); else wait_vm_bar<0>();

    static_assert(NSTAGE >= 3, "ring protocol needs 3 slots");
    s16x8 a[2][RB];
    s16x8 bA, bB;
    bA = ldb(smem, 0);
#pragma unroll
    for (int rb = 0; rb < RB; ++rb) a[0][rb] = *(const s16x8*)(smem + aoff + rb * 1024);
    bB = ldb(smem, NTW > 1 ? 1 : 0);

    unsigned cbuf = 0;
    constexpr int UN = (KS & 1) ? 2 : 1;
#pragma unroll 1
    for (int s0 = 0; s0 < nstages; s0 += UN) {
#pragma unroll
      for (int u = 0; u < UN; ++u) {
        const int s = s0 + u;
        if (s >= nstages) break;
        const bool do_issue = s + NSTAGE - 1 < nstages;
        const char* sb = smem + cbuf;
        cbuf = cbuf + STAGE == NSTAGE * STAGE ? 0u : cbuf + STAGE;
        const char* nb = smem + cbuf;
#pragma unroll
        for (int ks = 0; ks < KS; ++ks) {
          const int cur = (u * KS + ks) & 1;
          const char* sbk = sb + ks * BSTEP;
          const bool last = ks == KS - 1;
          const char* nbk = last ? nb : sb + (ks + 1) * BSTEP;
          const char* nak = last ? nb + aoff : sb + aoff + (ks + 1) * ASTEP;
          const bool has_next = !last || s + 1 < nstages;
          s16x8 bq[NTW + 2];
          bq[0] = bA; bq[1] = bB;
#pragma unroll
          for (int c = 0; c < NTW; ++c) {
            if (c + 2 < NTW) bq[c + 2] = ldb(sbk, c + 2);
            else {
              bq[c + 2] = bq[c];
              if (has_next) {
                if (c + 2 == NTW || NTW == 1) {
#pragma unroll
                  for (int rb = 0; rb < RB; ++rb) a[cur ^ 1][rb] = *(const s16x8*)(nak + rb * 1024);
                }
                bq[c + 2] = ldb(nbk, (c + 2 - NTW) % NTW);
              }
            }
            {
              constexpr int SL = (KS * NTW + 1) / 2;
              const int slot = ks * NTW + c;
#pragma unroll
              for (int j = 0; j < NGL; ++j)
                if ((j * SL) / NGL == slot && do_issue) issue1(j);
            }
            __builtin_amdgcn_sched_barrier(0);
#pragma unroll
            for (int rb = 0; rb < RB; ++rb)
              acc[rb][c] = __builtin_amdgcn_mfma_f32_16x16x32_bf16(bq[c], a[cur][rb], acc[rb][c], 0, 0, 0);
            __builtin_amdgcn_sched_barrier(0);
          }
          bA = bq[NTW]; bB = bq[NTW + 1];
        }
        if (do_issue) issue_done();
        if (s + 1 < nstages) {
          if (s + NSTAGE - 1 < nstages) wait_vm_bar<(NSTAGE - 3) * NGL>(); else wait_vm_bar<0>();
        }
      }
    }
  }
};

constexpr int ROWB = 48;
constexpr int CH_K = 0, CH_V = 256 * ROWB, CH_BYTES = 512 * ROWB;
constexpr int IMG_HEAD = 8 * CH_BYTES;

typedef Gemm<4, 2, 4, 11, 8, 1, 3> G1;
constexpr float QSCALE = 0.23570226039551584f * 1.4426950408889634f;

DI float silu_mul(float gt, float up) {
  const float e = __builtin_amdgcn_exp2f(gt * -1.4426950408889634f);
  return gt * __builtin_amdgcn_rcpf(1.0f + e) * up;
}

__global__ __launch_bounds__(512, 2) void k_gemm1(const char* __restrict__ xn, const u16* __restrict__ wqkv,
                                                  const u16* __restrict__ w1, const u16* __restrict__ w3,
                                                  char* __restrict__ hbuf, u16* __restrict__ qbuf,
                                                  char* __restrict__ img) {
  extern __shared__ __attribute__((aligned(16))) char smem[];
  const int b = blockIdx.x;
  const int cgp = (b & 7) * 4 + ((b >> 3) & 3);
  const int panel = b >> 5;
  const int m0 = panel * 256, n0 = cgp * 144;
  const int qt0 = (108 * cgp) >> 5, qt1 = (108 * (cgp + 1)) >> 5;
  const int aw = n0 & ~63, toff = (n0 & 63) >> 4;
  const int qaw = (16 * qt0) & ~63, qoff = qt0 & 3;
  const int w = __builtin_amdgcn_readfirstlane(threadIdx.x >> 6);
  const int wr = w >> 1, wc = w & 1;
  auto winptr = [&](int g, int& ld) -> const u16* {
    if (g < 3) { ld = F_; return w1 + aw + 64 * g; }
    if (g < 6) { ld = F_; return w3 + aw + 64 * (g - 3); }
    ld = QKV_;
    const int c = qaw + 64 * (g - 6);
    return wqkv + (c < QKV_ ? c : qaw);
  };
  auto tile = [&](int c) -> int {
    if (wc == 0) return c < 5 ? toff + c : c < 10 ? 12 + toff + (c - 5) : 24 + qoff;
    const int t = c < 4 ? toff + 5 + c : c < 8 ? 12 + toff + 1 + c : 24 + qoff + 1 + (c - 8);
    return t < 32 ? t : 31;
  };
  f32x4 acc[4][11];
  G1::run(smem, xn + (size_t)(m0 >> 4) * (D_ / 32) * 1024, (size_t)(D_ / 32) * 1024, D_ / 32, winptr, tile, acc);

  const int lane = threadIdx.x & 63, g = lane >> 4;
  const int row0 = m0 + 64 * wr + (lane & 15);
  if (wc == 0) {
#pragma unroll
    for (int c = 0; c < 5; ++c) {
      char* hp = hbuf + atile_off(row0, n0 + 16 * c + 4 * g, F_ / 32);
#pragma unroll
      for (int rb = 0; rb < 4; ++rb) {
        u32x2 o = {pk2(silu_mul(acc[rb][c][0], acc[rb][c + 5][0]), silu_mul(acc[rb][c][1], acc[rb][c + 5][1])),
                   pk2(silu_mul(acc[rb][c][2], acc[rb][c + 5][2]), silu_mul(acc[rb][c][3], acc[rb][c + 5][3]))};
        *(u32x2*)(hp + (size_t)rb * (F_ / 32) * 1024) = o;
      }
      __builtin_amdgcn_sched_barrier(0);
    }
  } else {
#pragma unroll
    for (int c = 0; c < 4; ++c) {
      char* hp = hbuf + atile_off(row0, n0 + 80 + 16 * c + 4 * g, F_ / 32);
#pragma unroll
      for (int rb = 0; rb < 4; ++rb) {
        u32x2 o = {pk2(silu_mul(acc[rb][c][0], acc[rb][c + 4][0]), silu_mul(acc[rb][c][1], acc[rb][c + 4][1])),
                   pk2(silu_mul(acc[rb][c][2], acc[rb][c + 4][2]), silu_mul(acc[rb][c][3], acc[rb][c + 4][3]))};
        *(u32x2*)(hp + (size_t)rb * (F_ / 32) * 1024) = o;
      }
      __builtin_amdgcn_sched_barrier(0);
    }
  }
#pragma unroll
  for (int c = 8; c < 11; ++c) {
    int t;
    if (wc == 0) { if (c != 10) continue; t = qt0; } else t = qt0 + 1 + (c - 8);
    if (t >= qt1) continue;
    const int which = t / 36;
    const int cc0 = 16 * (t - 36 * which) + 4 * g;
    const int head = cc0 / DH_, d0 = cc0 - head * DH_;
    const bool strad = d0 == 16;
    const int pos = d0 + 2 * (head & 1);
    const float sc = which == 0 ? QSCALE : 1.0f;
    char* base;
    size_t hstride;
    if (which == 0) { base = (char*)qbuf + (size_t)row0 * ROWB; hstride = (size_t)S_ * ROWB; }
    else { base = img + (row0 >> 8) * CH_BYTES + (which == 1 ? CH_K : CH_V) + (row0 & 255) * ROWB; hstride = IMG_HEAD; }
    char* p = base + head * hstride + pos * 2;
#pragma unroll
    for (int rb = 0; rb < 4; ++rb) {
      const unsigned lo = pk2(acc[rb][c][0] * sc, acc[rb][c][1] * sc), hi = pk2(acc[rb][c][2] * sc, acc[rb][c][3] * sc);
      u32x2 v = {lo, strad ? 0x3f803f80u : hi};
      *(u32x2*)(p + rb * 16 * ROWB) = v;
      if (strad) { u32x2 v2 = {0x3f803f80u, hi}; *(u32x2*)(base + (head + 1) * hstride + rb * 16 * ROWB) = v2; }
    }
  }
}

constexpr int AT_NBUF = 3;
constexpr int AT_LDS = AT_NBUF * CH_BYTES;
constexpr float AT_THR = 8.0f;

DI float max3f(float a, float b, float c) { return fmaxf(fmaxf(a, b), c); }
DI float rowmax16(const f32x16& s) {
  float mx = max3f(s[0], s[1], s[2]);
#pragma unroll
  for (int i = 3; i < 15; i += 2) mx = max3f(mx, s[i], s[i + 1]);
  return fmaxf(mx, s[15]);
}

DI void attn_block(char* smem, int ab, const char* __restrict__ qbuf, const char* __restrict__ img,
                   char* __restrict__ obuf) {
  const int tid = threadIdx.x, lane = tid & 63, r = lane & 31, hf = lane >> 5;
  const int w = __builtin_amdgcn_readfirstlane(tid >> 6);
  const int head = ab >> 3, qb = ab & 7, odd = head & 1;
  const unsigned lds0 = lds_base(smem);
  const char* gsrc = img + (size_t)head * IMG_HEAD + w * 3072;
  const unsigned vl = lane * 16;
  auto issue = [&](int ch) {
    const unsigned dst = lds0 + (unsigned)((ch % AT_NBUF) * CH_BYTES + w * 3072);
    const char* s = gsrc + (size_t)ch * CH_BYTES;
    glds16s(s, vl, dst);
    glds16s(s + 1024, vl, dst + 1024);
    glds16s(s + 2048, vl, dst + 2048);
  };
#pragma unroll
  for (int c = 0; c < AT_NBUF - 1; ++c) issue(c);

  const int qrow = qb * 256 + w * 32 + r;
  const char* qp = qbuf + ((size_t)head * S_ + qrow) * ROWB;
  u32x4 q0u = *(const u32x4*)(qp + 16 * hf);
  u32x4 q1u = {0u, 0u, 0u, 0u};
  if (hf == 0) { const u32x2 t2 = *(const u32x2*)(qp + 32); q1u[0] = t2[0]; q1u[1] = t2[1]; }

  const int kmo = CH_K + r * ROWB + hf * 16;
  const int kto = CH_K + r * ROWB + 32;
  const int vo = CH_V + (4 * hf + ((lane & 15) >> 2)) * ROWB + ((lane >> 4) & 1) * 32 + (lane & 3) * 8;
  const f32x16 zero16 = {0.f, 0.f, 0.f, 0.f, 0.f, 0.f, 0.f, 0.f, 0.f, 0.f, 0.f, 0.f, 0.f, 0.f, 0.f, 0.f};

  auto qk = [&](const char* cb, int t) -> f32x16 {
    const s16x8 kf0 = *(const s16x8*)(cb + kmo + t * 32 * ROWB);
    const u32x2 kt = *(const u32x2*)(cb + kto + t * 32 * ROWB);
    const u32x4 k1u = {kt[0], kt[1], 0u, 0u};
    f32x16 s = __builtin_amdgcn_mfma_f32_32x32x16_bf16(kf0, __builtin_bit_cast(s16x8, q0u), zero16, 0, 0, 0);
    return __builtin_amdgcn_mfma_f32_32x32x16_bf16(__builtin_bit_cast(s16x8, k1u), __builtin_bit_cast(s16x8, q1u), s, 0, 0, 0);
  };
  auto vfrag = [&](const char* cb, int st) -> s16x8 {
    const char* p = cb + vo + st * 16 * ROWB;
    const s16x4 lo = __builtin_amdgcn_ds_read_tr16_b64_v4i16((lds_s16x4_ptr)(p));
    const s16x4 hi = __builtin_amdgcn_ds_read_tr16_b64_v4i16((lds_s16x4_ptr)(p + 8 * ROWB));
    return __builtin_shufflevector(lo, hi, 0, 1, 2, 3, 4, 5, 6, 7);
  };

  f32x16 o = zero16;
  float mrun = 0.f;
  auto set_m = [&](float m) {
    const float mh = bfr(m), ml = bfr(m - mh);
    mrun = mh + ml;
    const unsigned mm = pk2(-mh, -ml);
    if (hf == 0) { if (odd) q0u[0] = mm; else q1u[1] = mm; }
  };
  set_m(0.f);

#pragma unroll 1
  for (int ch = 0; ch < 8; ++ch) {
    if (ch + AT_NBUF - 2 < 8) wait_vm_bar<(AT_NBUF - 2) * 3>(); else wait_vm_bar<0>();
    if (ch + AT_NBUF - 1 < 8) issue(ch + AT_NBUF - 1);
    const char* cb = smem + (ch % AT_NBUF) * CH_BYTES;
    f32x16 sc = qk(cb, 0);
    {
      const float mx = rowmax16(sc);
      if (ch == 0 || __any(mx > AT_THR)) {
        float mq = fmaxf(mx, __shfl_xor(mx, 32));
        if (ch != 0) mq = fmaxf(mq, 0.f);
        const float mold = mrun;
        set_m(mold + mq);
        const float dl = mrun - mold;
        const float al = __builtin_amdgcn_exp2f(-dl);
#pragma unroll
        for (int i = 0; i < 16; ++i) { sc[i] -= dl; o[i] *= al; }
      }
    }
#pragma unroll
    for (int t = 0; t < 8; ++t) {
      f32x16 sn;
      if (t + 1 < 8) sn = qk(cb, t + 1);
      const s16x8 vf0 = vfrag(cb, 2 * t);
      const s16x8 vf1 = vfrag(cb, 2 * t + 1);
      u32x4 p0, p1;
#pragma unroll
      for (int i = 0; i < 4; ++i) p0[i] = pk2(__builtin_amdgcn_exp2f(sc[2 * i]), __builtin_amdgcn_exp2f(sc[2 * i + 1]));
      o = __builtin_amdgcn_mfma_f32_32x32x16_bf16(vf0, __builtin_bit_cast(s16x8, p0), o, 0, 0, 0);
#pragma unroll
      for (int i = 0; i < 4; ++i) p1[i] = pk2(__builtin_amdgcn_exp2f(sc[8 + 2 * i]), __builtin_amdgcn_exp2f(sc[8 + 2 * i + 1]));
      o = __builtin_amdgcn_mfma_f32_32x32x16_bf16(vf1, __builtin_bit_cast(s16x8, p1), o, 0, 0, 0);
      if (t + 1 < 8) {
        const float mx = rowmax16(sn);
        if (__any(mx > AT_THR)) {
          const float mq = fmaxf(fmaxf(mx, __shfl_xor(mx, 32)), 0.f);
          const float mold = mrun;
          set_m(mold + mq);
          const float dl = mrun - mold;
          const float al = __builtin_amdgcn_exp2f(-dl);
#pragma unroll
          for (int i = 0; i < 16; ++i) { sn[i] -= dl; o[i] *= al; }
        }
        sc = sn;
      }
    }
  }
  float l = odd ? o[0] : o[10];
  l = __shfl(l, r);
  const float inv = 1.0f / l;
  const int c0 = head * DH_ + 4 * hf - 2 * odd;
  if (!(odd && hf == 0)) *(unsigned*)(obuf + atile_off(qrow, c0 + 0, D_ / 32)) = pk2(o[0] * inv, o[1] * inv);
  *(unsigned*)(obuf + atile_off(qrow, c0 + 2, D_ / 32)) = pk2(o[2] * inv, o[3] * inv);
  *(unsigned*)(obuf + atile_off(qrow, c0 + 8, D_ / 32)) = pk2(o[4] * inv, o[5] * inv);
  *(unsigned*)(obuf + atile_off(qrow, c0 + 10, D_ / 32)) = pk2(o[6] * inv, o[7] * inv);
  if (hf == 0) {
    *(unsigned*)(obuf + atile_off(qrow, c0 + 16, D_ / 32)) = pk2(o[8] * inv, o[9] * inv);
    if (odd) *(unsigned*)(obuf + atile_off(qrow, c0 + 18, D_ / 32)) = pk2(o[10] * inv, o[11] * inv);
  }
}

constexpr int DSPLIT = 5;
typedef Gemm<2, 4, 4, 3, 3, 1, 4> G2;
DI void down_block(char* smem, int b, const char* __restrict__ hbuf, const u16* __restrict__ w2,
                   float* __restrict__ part) {
  const int split = b % DSPLIT, r1 = b / DSPLIT, cg = r1 % 3, panel = r1 / 3;
  const int c0 = (72 * split) / DSPLIT, c1 = (72 * (split + 1)) / DSPLIT;
  const int m0 = panel * 128, n0 = cg * 192, k0 = c0 * 64;
  const int w = __builtin_amdgcn_readfirstlane(threadIdx.x >> 6);
  const int wr = w >> 2, wc = w & 3;
  auto winptr = [&](int g, int& ld) -> const u16* { ld = D_; return w2 + (size_t)k0 * D_ + n0 + 64 * g; };
  auto tile = [&](int c) -> int { return wc * 3 + c; };
  f32x4 acc[4][3];
  G2::run(smem, hbuf + ((size_t)(m0 >> 4) * (F_ / 32) + (k0 >> 5)) * 1024, (size_t)(F_ / 32) * 1024, 2 * (c1 - c0), winptr, tile, acc);
  const int lane = threadIdx.x & 63, g = lane >> 4;
#pragma unroll
  for (int rb = 0; rb < 4; ++rb) {
    const int row = m0 + 64 * wr + 16 * rb + (lane & 15);
    float* pr = part + ((size_t)split * S_ + row) * D_ + n0 + 48 * wc + 4 * g;
#pragma unroll
    for (int c = 0; c < 3; ++c) *(f32x4*)(pr + 16 * c) = acc[rb][c];
  }
}

constexpr int MID_LDS = AT_LDS > G2::LDS ? AT_LDS : G2::LDS;
__global__ __launch_bounds__(512, 4) void k_mid(const char* __restrict__ qbuf, const char* __restrict__ img,
                                                char* __restrict__ obuf, const char* __restrict__ hbuf,
                                                const u16* __restrict__ w2, float* __restrict__ part) {
  extern __shared__ __attribute__((aligned(16))) char smem[];
  if (blockIdx.x < 256) attn_block(smem, blockIdx.x, qbuf, img, obuf);
  else down_block(smem, blockIdx.x - 256, hbuf, w2, part);
}

typedef Gemm<2, 2, 2, 2, 1, 2, 3> G3;
__global__ __launch_bounds__(256, 2) void k_final(const char* __restrict__ obuf, const u16* __restrict__ wo,
                                                  const float* __restrict__ part, const u16* __restrict__ x,
                                                  u16* __restrict__ out) {
  extern __shared__ __attribute__((aligned(16))) char smem[];
  const int b = blockIdx.x;
  const int cg = b % 9, panel = b / 9;
  const int m0 = panel * 64, n0 = cg * 64;
  const int lane = threadIdx.x & 63, g = lane >> 4;
  const int w = __builtin_amdgcn_readfirstlane(threadIdx.x >> 6);
  const int wr = w >> 1, wc = w & 1;
  f32x4 base[2][2];
#pragma unroll
  for (int rb = 0; rb < 2; ++rb)
#pragma unroll
    for (int c = 0; c < 2; ++c) {
      const int row = m0 + 32 * wr + 16 * rb + (lane & 15), col = n0 + 32 * wc + 16 * c + 4 * g;
      f32x4 f = {0.f, 0.f, 0.f, 0.f};
#pragma unroll
      for (int sp = 0; sp < DSPLIT; ++sp) f += *(const f32x4*)(part + ((size_t)sp * S_ + row) * D_ + col);
      base[rb][c] = f;
    }
  auto winptr = [&](int g_, int& ld) -> const u16* { ld = D_; return wo + n0; };
  auto tile = [&](int c) -> int { return wc * 2 + c; };
  f32x4 acc[2][2];
  G3::run(smem, obuf + (size_t)(m0 >> 4) * (D_ / 32) * 1024, (size_t)(D_ / 32) * 1024, D_ / 64, winptr, tile, acc);
#pragma unroll
  for (int rb = 0; rb < 2; ++rb) {
#pragma unroll
    for (int c = 0; c < 2; ++c) {
      const int row = m0 + 32 * wr + 16 * rb + (lane & 15), col = n0 + 32 * wc + 16 * c + 4 * g;
      const u32x2 xv = *(const u32x2*)(x + (size_t)row * D_ + col);
      float xf[4] = {__uint_as_float(xv[0] << 16), __uint_as_float(xv[0] & 0xffff0000u),
                     __uint_as_float(xv[1] << 16), __uint_as_float(xv[1] & 0xffff0000u)};
      float r4[4];
#pragma unroll
      for (int i = 0; i < 4; ++i) r4[i] = bfr(xf[i] + bfr(acc[rb][c][i])) + bfr(base[rb][c][i]);
      u32x2 o = {pk2(r4[0], r4[1]), pk2(r4[2], r4[3])};
      *(u32x2*)(out + (size_t)row * D_ + col) = o;
    }
  }
}

constexpr size_t WS_XN = 0;
constexpr size_t WS_H = WS_XN + (size_t)S_ * D_ * 2;
constexpr size_t WS_Q = WS_H + (size_t)S_ * F_ * 2;
constexpr size_t WS_IMG = WS_Q + (size_t)H_ * S_ * ROWB;
constexpr size_t WS_O = WS_IMG + (size_t)H_ * IMG_HEAD;
constexpr size_t WS_P = WS_O + (size_t)S_ * D_ * 2;
constexpr size_t WS_END = WS_P + (size_t)DSPLIT * S_ * D_ * 4;

extern "C" void kernel_launch(void* const* d_in, const int* in_sizes, int n_in, void* d_out, int out_size,
                              void* d_ws, size_t ws_size, hipStream_t stream) {
  const u16* x = (const u16*)d_in[0];
  const u16* wqkv = (const u16*)d_in[1];
  const u16* wo = (const u16*)d_in[2];
  const u16* w1 = (const u16*)d_in[4];
  const u16* w3 = (const u16*)d_in[5];
  const u16* w2 = (const u16*)d_in[6];
  char* ws = (char*)d_ws;
  char* xn = ws + WS_XN;
  char* hbuf = ws + WS_H;
  u16* qbuf = (u16*)(ws + WS_Q);
  char* img = ws + WS_IMG;
  char* obuf = ws + WS_O;
  float* part = (float*)(ws + WS_P);
  u16* out = (u16*)d_out;

  (void)hipFuncSetAttribute((const void*)k_gemm1, hipFuncAttributeMaxDynamicSharedMemorySize, G1::LDS);
  (void)hipFuncSetAttribute((const void*)k_mid, hipFuncAttributeMaxDynamicSharedMemorySize, MID_LDS);
  (void)hipFuncSetAttribute((const void*)k_final, hipFuncAttributeMaxDynamicSharedMemorySize, G3::LDS);

  k_ln<<<S_ / 4, 256, 0, stream>>>(x, xn);
  k_gemm1<<<256, 512, G1::LDS, stream>>>(xn, wqkv, w1, w3, hbuf, qbuf, img);
  k_mid<<<256 + 16 * 3 * DSPLIT, 512, MID_LDS, stream>>>((const char*)qbuf, img, obuf, hbuf, w2, part);
  k_final<<<32 * 9, 256, G3::LDS, stream>>>(obuf, wo, part, x, out);
}
```

```cpp
#include <hip/hip_runtime.h>
#include <hip/hip_bf16.h>
#include <stdint.h>

typedef __attribute__((ext_vector_type(4))) short s16x4;
typedef __attribute__((ext_vector_type(8))) short s16x8;
typedef __attribute__((ext_vector_type(4))) float f32x4;
typedef __attribute__((ext_vector_type(16))) float f32x16;
typedef __attribute__((ext_vector_type(2))) unsigned u32x2;
typedef __attribute__((ext_vector_type(4))) unsigned u32x4;
typedef __attribute__((ext_vector_type(2))) __bf16 bf16x2;
typedef unsigned short u16;
typedef __attribute__((address_space(3))) s16x4* lds_s16x4_ptr;
typedef __attribute__((address_space(3))) char* lds_char_ptr;

constexpr int S_ = 2048, D_ = 576, H_ = 32, DH_ = 18, F_ = 4608, QKV_ = 1728;

#define DI __device__ __forceinline__

DI float bf2f(u16 v) { return __uint_as_float(((unsigned)v) << 16); }
DI u16 f2bf(float f) { return __builtin_bit_cast(u16, (__bf16)f); }
DI float bfr(float f) { return bf2f(f2bf(f)); }
DI unsigned pk2(float a, float b) { bf16x2 v = {(__bf16)a, (__bf16)b}; return __builtin_bit_cast(unsigned, v); }

DI void glds16(const void* gsrc, unsigned lds_dst) {
  unsigned keep;
  asm volatile("s_mov_b32 %0, m0\n\ts_mov_b32 m0, %2\n\ts_nop 0\n\tglobal_load_lds_dwordx4 %1, off\n\ts_mov_b32 m0, %0"
               : "=&s"(keep) : "v"(gsrc), "s"(lds_dst) : "memory");
}
DI void glds16s(const void* sbase, unsigned voff, unsigned lds_dst) {
  unsigned keep;
  asm volatile("s_mov_b32 %0, m0\n\ts_mov_b32 m0, %3\n\ts_nop 0\n\tglobal_load_lds_dwordx4 %1, %2\n\ts_mov_b32 m0, %0"
               : "=&s"(keep) : "v"(voff), "s"(sbase), "s"(lds_dst) : "memory");
}
template <int N> DI void wait_vm_bar() { asm volatile("s_waitcnt vmcnt(%0)\n\ts_barrier" ::"i"(N) : "memory"); }

DI unsigned lds_base(char* smem) { return (unsigned)(uintptr_t)(lds_char_ptr)smem; }

DI size_t atile_off(int row, int k, int nks) {
  const int r = row & 15, ch = (k >> 3) & 3;
  return ((size_t)(row >> 4) * nks + (k >> 5)) * 1024 + r * 64 + ((ch ^ ((-(r >> 2)) & 3)) << 4) + (k & 7) * 2;
}

__global__ __launch_bounds__(256) void k_ln(const u16* __restrict__ x, char* __restrict__ xn) {
  const int row = blockIdx.x * 4 + (threadIdx.x >> 6), lane = threadIdx.x & 63;
  const u16* xr = x + (size_t)row * D_;
  u32x4 c0 = *(const u32x4*)(xr + lane * 8);
  u32x4 c1 = {0u, 0u, 0u, 0u};
  const bool two = lane < 8;
  if (two) c1 = *(const u32x4*)(xr + 512 + lane * 8);
  float v[16];
#pragma unroll
  for (int i = 0; i < 4; ++i) {
    v[2 * i] = __uint_as_float(c0[i] << 16); v[2 * i + 1] = __uint_as_float(c0[i] & 0xffff0000u);
    v[8 + 2 * i] = __uint_as_float(c1[i] << 16); v[8 + 2 * i + 1] = __uint_as_float(c1[i] & 0xffff0000u);
  }
  float s = 0.f;
#pragma unroll
  for (int i = 0; i < 16; ++i) s += v[i];
#pragma unroll
  for (int o = 32; o > 0; o >>= 1) s += __shfl_xor(s, o);
  const float mean = s * (1.0f / D_);
  float q = 0.f;
#pragma unroll
  for (int i = 0; i < 8; ++i) { float d = v[i] - mean; q += d * d; }
  if (two) {
#pragma unroll
    for (int i = 8; i < 16; ++i) { float d = v[i] - mean; q += d * d; }
  }
#pragma unroll
  for (int o = 32; o > 0; o >>= 1) q += __shfl_xor(q, o);
  const float var = bfr(q * (1.0f / D_));
  const float m = bfr(mean);
  const float sd = bfr(sqrtf(bfr(var + 1e-5f)));
  u32x4 o0, o1;
#pragma unroll
  for (int i = 0; i < 4; ++i) {
    o0[i] = pk2(bfr(v[2 * i] - m) / sd, bfr(v[2 * i + 1] - m) / sd);
    o1[i] = pk2(bfr(v[8 + 2 * i] - m) / sd, bfr(v[8 + 2 * i + 1] - m) / sd);
  }
  *(u32x4*)(xn + atile_off(row, lane * 8, D_ / 32)) = o0;
  if (two) *(u32x4*)(xn + atile_off(row, 512 + lane * 8, D_ / 32)) = o1;
}

template <int WR, int WC, int RB, int NTW, int NG, int NSTAGE, int LW, bool XPF, int PFD, bool XL>
struct Gemm {
  static constexpr int NW = WR * WC;
  static constexpr int ROWS = WR * RB * 16;
  static constexpr int BSTEP = 4 * NG * 1024;
  static constexpr int ASTEP = ROWS * 64;
  static constexpr int STAGE = BSTEP + ASTEP;
  static constexpr int LDS = STAGE * NSTAGE;
  static constexpr int NBT = 4 * NG, NAT = ROWS / 16, NI = NBT + NAT;
  static constexpr int NGL = (NI + LW - 1) / LW;
  static constexpr int AHEAD = XPF ? 1 : 0;
  static_assert(NSTAGE >= 2 + AHEAD, "ring too short");
  static constexpr int TW = NW - LW;
  static constexpr int NTI = PFD > 0 ? (NI * 8 + 64 * TW - 1) / (64 * TW) : 0;

  template <typename WinFn, typename TileFn, typename UseFn>
  static DI void run(char* smem, const char* __restrict__ A, size_t rbs, int nk, WinFn winptr, TileFn tile, UseFn used,
                     f32x4 (&acc)[RB][NTW]) {
    const int tid = threadIdx.x, lane = tid & 63;
    const int w = __builtin_amdgcn_readfirstlane(tid >> 6);
    const int wr = w / WC;
    const bool loader = XL ? w >= NW : w < LW;
    const int lw = XL ? (w >= NW ? w - NW : 0) : w % LW;
    const unsigned lds0 = lds_base(smem);
    const char* src[NGL]; int sstep[NGL]; unsigned sdst[NGL];
    unsigned vob[2][2];
    unsigned onmask = 0;
    int kind[NGL];
    const int brow = lane >> 3, bip = (lane & 7) >> 1, bhalf = lane & 1;
    int ld0 = 0;
    (void)winptr(0, ld0);
    int ld1 = ld0;
    (void)winptr(NG - 1, ld1);
#pragma unroll
    for (int kp = 0; kp < 2; ++kp) {
      const int swz = ((brow >> 1) & 1) | (kp << 1);
      vob[kp][0] = (unsigned)(brow * ld0 + 16 * (bip ^ swz) + 8 * bhalf) * 2u;
      vob[kp][1] = (unsigned)(brow * ld1 + 16 * (bip ^ swz) + 8 * bhalf) * 2u;
    }
    const unsigned voa = (unsigned)lane * 16u;
#pragma unroll
    for (int j = 0; j < NGL; ++j) {
      int id = lw + LW * j;
      if (id >= NI) id -= NI;
      if (id < NBT) {
        const int kb = id / NG, grp = id % NG;
        const int swz = ((brow >> 1) & 1) | ((kb & 1) << 1);
        int ld;
        const u16* p = winptr(grp, ld);
        src[j] = (const char*)(p + (size_t)(kb * 8) * ld);
        kind[j] = (kb & 1) * 2 + (ld == ld0 ? 0 : 1);
        if (used(grp * 4 + (bip ^ swz)) || lane == 0) onmask |= 1u << j;
        sstep[j] = 32 * ld * 2;
        sdst[j] = (unsigned)id * 1024u;
      } else {
        const int rb = id - NBT;
        src[j] = A + rb * rbs;
        kind[j] = 4;
        onmask |= 1u << j;
        sstep[j] = 1024;
        sdst[j] = (unsigned)(BSTEP + rb * 1024);
      }
    }
    const char* tp[NTI > 0 ? NTI : 1]; int tstep[NTI > 0 ? NTI : 1];
    if constexpr (PFD > 0) {
#pragma unroll
      for (int jt = 0; jt < NTI; ++jt) {
        int id = ((w - LW + TW) % TW + TW * jt) * 8 + (lane >> 3);
        id = id < NI ? id : NI - 1;
        const int ln = lane & 7;
        if (id < NBT) {
          int ld;
          const u16* p = winptr(id % NG, ld);
          tp[jt] = (const char*)(p + (size_t)((id / NG) * 8 + ln) * ld);
          tstep[jt] = 32 * ld * 2;
        } else {
          tp[jt] = A + (id - NBT) * rbs + ln * 128;
          tstep[jt] = 1024;
        }
        tp[jt] += (size_t)(NSTAGE - 1) * tstep[jt];
      }
    }
    unsigned sink = 0;
    auto touch = [&]() {
#pragma unroll
      for (int jt = 0; jt < NTI; ++jt) {
        asm volatile("global_load_dword %0, %1, off" : "+v"(sink) : "v"(tp[jt]) : "memory");
        tp[jt] += tstep[jt];
      }
    };
    unsigned ibuf = 0;
    auto issue1 = [&](int j) {
      const int kd = kind[j];
      const unsigned vo = kd == 4 ? voa : kd == 0 ? vob[0][0] : kd == 1 ? vob[0][1] : kd == 2 ? vob[1][0] : vob[1][1];
      if ((onmask >> j) & 1u) glds16s(src[j], vo, lds0 + ibuf + sdst[j]);
      src[j] += sstep[j];
    };
    auto issue_done = [&]() { ibuf = ibuf + STAGE == LDS ? 0u : ibuf + STAGE; };
    const int g = lane >> 4, q = (lane & 15) >> 2, p4 = lane & 3;
    const int bswz = ((q >> 1) & 1) | ((g & 1) << 1);
    int bo[NTW];
#pragma unroll
    for (int c = 0; c < NTW; ++c) {
      const int t = tile(c);
      bo[c] = g * NG * 1024 + q * 128 + (((t & 3) ^ bswz) * 32) + p4 * 8 + (t >> 2) * 1024;
    }
    const int ar = lane & 15;
    const int aoff = BSTEP + wr * RB * 1024 + ar * 64 + ((g ^ ((-(ar >> 2)) & 3)) * 16);

#pragma unroll
    for (int rb = 0; rb < RB; ++rb)
#pragma unroll
      for (int c = 0; c < NTW; ++c) acc[rb][c] = f32x4{0.f, 0.f, 0.f, 0.f};

    auto ldb = [&](const char* sbk, int c) -> s16x8 {
      const char* bp = sbk + bo[c];
      const s16x4 b0 = __builtin_amdgcn_ds_read_tr16_b64_v4i16((lds_s16x4_ptr)(bp));
      const s16x4 b1 = __builtin_amdgcn_ds_read_tr16_b64_v4i16((lds_s16x4_ptr)(bp + 512));
      return __builtin_shufflevector(b0, b1, 0, 1, 2, 3, 4, 5, 6, 7);
    };
    auto lda = [&](const char* sbk, s16x8 (&dst)[RB]) {
#pragma unroll
      for (int rb = 0; rb < RB; ++rb) dst[rb] = *(const s16x8*)(sbk + aoff + rb * 1024);
    };

    if (loader) {
#pragma unroll
      for (int s = 0; s < NSTAGE - 1; ++s) {
#pragma unroll
        for (int j = 0; j < NGL; ++j) issue1(j);
        issue_done();
      }
    } else if constexpr (PFD > 0) {
      for (int s = NSTAGE - 1; s < NSTAGE - 1 + PFD && s < nk; ++s) touch();
    }
    wait_vm_bar<(NSTAGE - 2 - AHEAD) * NGL>();
    if (XL && loader) {
#pragma unroll 1
      for (int s = 0; s + 1 < nk; ++s) {
        if (s + NSTAGE - 1 < nk) {
#pragma unroll
          for (int j = 0; j < NGL; ++j) issue1(j);
          issue_done();
          wait_vm_bar<(NSTAGE - 2 - AHEAD) * NGL>();
        } else wait_vm_bar<0>();
      }
      return;
    }
    s16x8 a[2][RB];
    s16x8 bA = ldb(smem, 0);
    lda(smem, a[0]);
    s16x8 bB = ldb(smem, NTW > 1 ? 1 : 0);

    unsigned cbuf = 0;
#pragma unroll 1
    for (int s0 = 0; s0 < nk; s0 += 2) {
#pragma unroll
      for (int u = 0; u < 2; ++u) {
        const int s = s0 + u;
        if (s >= nk) break;
        const bool do_issue = loader && s + NSTAGE - 1 < nk, has_next = s + 1 < nk;
        const char* sb = smem + cbuf;
        cbuf = cbuf + STAGE == LDS ? 0u : cbuf + STAGE;
        const char* nb = smem + cbuf;
        if (do_issue) {
#pragma unroll
          for (int j = 0; j < NGL; ++j) issue1(j);
          issue_done();
        }
        if constexpr (PFD > 0) { if (!loader && s + NSTAGE - 1 + PFD < nk) touch(); }
        s16x8 bq[NTW + 2];
        bq[0] = bA; bq[1] = bB;
#pragma unroll
        for (int c = 0; c < NTW; ++c) {
          if (c + 2 < NTW) bq[c + 2] = ldb(sb, c + 2);
          else {
            bq[c + 2] = bq[c];
            if (XPF && has_next) {
              if (c + 2 == NTW || NTW == 1) lda(nb, a[u ^ 1]);
              bq[c + 2] = ldb(nb, (c + 2 - NTW) % NTW);
            }
          }
          __builtin_amdgcn_sched_barrier(0);
#pragma unroll
          for (int rb = 0; rb < RB; ++rb)
            acc[rb][c] = __builtin_amdgcn_mfma_f32_16x16x32_bf16(bq[c], a[u][rb], acc[rb][c], 0, 0, 0);
          __builtin_amdgcn_sched_barrier(0);
        }
        bA = bq[NTW]; bB = bq[NTW + 1];
        if (has_next) {
          if (s + NSTAGE - 1 < nk) wait_vm_bar<(NSTAGE - 2 - AHEAD) * NGL>(); else wait_vm_bar<0>();
          if (!XPF) {
            bA = ldb(nb, 0);
            lda(nb, a[u ^ 1]);
            bB = ldb(nb, NTW > 1 ? 1 : 0);
          }
        }
      }
    }
    if constexpr (PFD > 0) asm volatile("s_waitcnt vmcnt(0)" : "+v"(sink) : : "memory");
  }
};

constexpr int ROWB = 48;
constexpr int CH_K = 0, CH_V = 256 * ROWB, CH_BYTES = 512 * ROWB;
constexpr int IMG_HEAD = 8 * CH_BYTES;

typedef Gemm<4, 2, 4, 11, 8, 3, 4, true, 0, false> G1;
constexpr float QSCALE = 0.23570226039551584f * 1.4426950408889634f;

DI float silu_mul(float gt, float up) {
  const float e = __builtin_amdgcn_exp2f(gt * -1.4426950408889634f);
  return gt * __builtin_amdgcn_rcpf(1.0f + e) * up;
}

__global__ __launch_bounds__(512, 2) void k_gemm1(const char* __restrict__ xn, const u16* __restrict__ wqkv,
                                                  const u16* __restrict__ w1, const u16* __restrict__ w3,
                                                  char* __restrict__ hbuf, u16* __restrict__ qbuf,
                                                  char* __restrict__ img) {
  extern __shared__ __attribute__((aligned(16))) char smem[];
  const int b = blockIdx.x;
  const int cgp = (b & 7) * 4 + ((b >> 3) & 3);
  const int panel = b >> 5;
  const int m0 = panel * 256, n0 = cgp * 144;
  const int qt0 = (108 * cgp) >> 5, qt1 = (108 * (cgp + 1)) >> 5;
  const int aw = n0 & ~63, toff = (n0 & 63) >> 4;
  const int qaw = (16 * qt0) & ~63, qoff = qt0 & 3;
  const int w = __builtin_amdgcn_readfirstlane(threadIdx.x >> 6);
  const int wr = w >> 1, wc = w & 1;
  auto winptr = [&](int g, int& ld) -> const u16* {
    if (g < 3) { ld = F_; return w1 + aw + 64 * g; }
    if (g < 6) { ld = F_; return w3 + aw + 64 * (g - 3); }
    ld = QKV_;
    const int c = qaw + 64 * (g - 6);
    return wqkv + (c < QKV_ ? c : qaw);
  };
  auto tile = [&](int c) -> int {
    if (wc == 0) return c < 5 ? toff + c : c < 10 ? 12 + toff + (c - 5) : 24 + qoff;
    const int t = c < 4 ? toff + 5 + c : c < 8 ? 12 + toff + 1 + c : 24 + qoff + 1 + (c - 8);
    return t < 32 ? t : 31;
  };
  auto used = [&](int t) -> bool {
    return t < 12 ? (unsigned)(t - toff) < 9u : t < 24 ? (unsigned)(t - 12 - toff) < 9u : (unsigned)(t - 24 - qoff) < (unsigned)(qt1 - qt0);
  };
  f32x4 acc[4][11];
  G1::run(smem, xn + (size_t)(m0 >> 4) * (D_ / 32) * 1024, (size_t)(D_ / 32) * 1024, D_ / 32, winptr, tile, used, acc);

  const int lane = threadIdx.x & 63, g = lane >> 4;
  const int row0 = m0 + 64 * wr + (lane & 15);
  if (wc == 0) {
#pragma unroll
    for (int c = 0; c < 5; ++c) {
      char* hp = hbuf + atile_off(row0, n0 + 16 * c + 4 * g, F_ / 32);
#pragma unroll
      for (int rb = 0; rb < 4; ++rb) {
        u32x2 o = {pk2(silu_mul(acc[rb][c][0], acc[rb][c + 5][0]), silu_mul(acc[rb][c][1], acc[rb][c + 5][1])),
                   pk2(silu_mul(acc[rb][c][2], acc[rb][c + 5][2]), silu_mul(acc[rb][c][3], acc[rb][c + 5][3]))};
        *(u32x2*)(hp + (size_t)rb * (F_ / 32) * 1024) = o;
      }
      __builtin_amdgcn_sched_barrier(0);
    }
  } else {
#pragma unroll
    for (int c = 0; c < 4; ++c) {
      char* hp = hbuf + atile_off(row0, n0 + 80 + 16 * c + 4 * g, F_ / 32);
#pragma unroll
      for (int rb = 0; rb < 4; ++rb) {
        u32x2 o = {pk2(silu_mul(acc[rb][c][0], acc[rb][c + 4][0]), silu_mul(acc[rb][c][1], acc[rb][c + 4][1])),
                   pk2(silu_mul(acc[rb][c][2], acc[rb][c + 4][2]), silu_mul(acc[rb][c][3], acc[rb][c + 4][3]))};
        *(u32x2*)(hp + (size_t)rb * (F_ / 32) * 1024) = o;
      }
      __builtin_amdgcn_sched_barrier(0);
    }
  }
#pragma unroll
  for (int c = 8; c < 11; ++c) {
    int t;
    if (wc == 0) { if (c != 10) continue; t = qt0; } else t = qt0 + 1 + (c - 8);
    if (t >= qt1) continue;
    const int which = t / 36;
    const int cc0 = 16 * (t - 36 * which) + 4 * g;
    const int head = cc0 / DH_, d0 = cc0 - head * DH_;
    const bool strad = d0 == 16;
    const int pos = d0 + 2 * (head & 1);
    const float sc = which == 0 ? QSCALE : 1.0f;
    char* base;
    size_t hstride;
    if (which == 0) { base = (char*)qbuf + (size_t)row0 * ROWB; hstride = (size_t)S_ * ROWB; }
    else { base = img + (row0 >> 8) * CH_BYTES + (which == 1 ? CH_K : CH_V) + (row0 & 255) * ROWB; hstride = IMG_HEAD; }
    char* p = base + head * hstride + pos * 2;
#pragma unroll
    for (int rb = 0; rb < 4; ++rb) {
      const unsigned lo = pk2(acc[rb][c][0] * sc, acc[rb][c][1] * sc), hi = pk2(acc[rb][c][2] * sc, acc[rb][c][3] * sc);
      u32x2 v = {lo, strad ? 0x3f803f80u : hi};
      *(u32x2*)(p + rb * 16 * ROWB) = v;
      if (strad) { u32x2 v2 = {0x3f803f80u, hi}; *(u32x2*)(base + (head + 1) * hstride + rb * 16 * ROWB) = v2; }
    }
  }
}

constexpr int AT_LDS = 3 * CH_BYTES;
constexpr float AT_GAP = 7.0f;

DI float max3f(float a, float b, float c) { return fmaxf(fmaxf(a, b), c); }
DI float rowmax16(const f32x16& s) {
  float mx = max3f(s[0], s[1], s[2]);
#pragma unroll
  for (int i = 3; i < 15; i += 2) mx = max3f(mx, s[i], s[i + 1]);
  return fmaxf(mx, s[15]);
}

template <int KG, int AT_NBUF>
DI void attn_block(char* smem, int ab, const char* __restrict__ qbuf, const char* __restrict__ img,
                   char* __restrict__ obuf) {
  const int tid = threadIdx.x, lane = tid & 63, r = lane & 31, hf = lane >> 5;
  const int wv = __builtin_amdgcn_readfirstlane(tid >> 6);
  const int w = wv & 7, kg = wv >> 3;
  constexpr int NT = 8 / KG, NP = 3 / KG + (3 % KG ? 1 : 0);
  const int head = (ab & 7) * 4 + ((ab >> 3) & 3), qb = ab >> 5, odd = head & 1;
  const unsigned lds0 = lds_base(smem);
  const char* gsrc = img + (size_t)head * IMG_HEAD;
  const unsigned vl = lane * 16;
  auto issue = [&](int ch) {
    const unsigned dst = lds0 + (unsigned)((ch % AT_NBUF) * CH_BYTES);
    const char* s = gsrc + (size_t)ch * CH_BYTES;
#pragma unroll
    for (int j = 0; j < NP; ++j) {
      int pc = wv + 8 * KG * j;
      if (pc >= 24) pc -= 24;
      glds16s(s + pc * 1024, vl, dst + pc * 1024);
    }
  };
#pragma unroll
  for (int c = 0; c < AT_NBUF - 1; ++c) issue(c);

  const int qrow = qb * 256 + w * 32 + r;
  const char* qp = qbuf + ((size_t)head * S_ + qrow) * ROWB;
  u32x4 q0u = *(const u32x4*)(qp + 16 * hf);
  u32x4 q1u = {0u, 0u, 0u, 0u};
  if (hf == 0) { const u32x2 t2 = *(const u32x2*)(qp + 32); q1u[0] = t2[0]; q1u[1] = t2[1]; }

  const int kmo = CH_K + r * ROWB + hf * 16;
  const int kto = CH_K + r * ROWB + 32;
  const int vo = CH_V + (4 * hf + ((lane & 15) >> 2)) * ROWB + ((lane >> 4) & 1) * 32 + (lane & 3) * 8;
  const f32x16 zero16 = {0.f, 0.f, 0.f, 0.f, 0.f, 0.f, 0.f, 0.f, 0.f, 0.f, 0.f, 0.f, 0.f, 0.f, 0.f, 0.f};

  auto qk = [&](const char* cb, int t) -> f32x16 {
    const s16x8 kf0 = *(const s16x8*)(cb + kmo + t * 32 * ROWB);
    const u32x2 kt = *(const u32x2*)(cb + kto + t * 32 * ROWB);
    const u32x4 k1u = {kt[0], kt[1], 0u, 0u};
    f32x16 s = __builtin_amdgcn_mfma_f32_32x32x16_bf16(kf0, __builtin_bit_cast(s16x8, q0u), zero16, 0, 0, 0);
    return __builtin_amdgcn_mfma_f32_32x32x16_bf16(__builtin_bit_cast(s16x8, k1u), __builtin_bit_cast(s16x8, q1u), s, 0, 0, 0);
  };
  auto vfrag = [&](const char* cb, int st) -> s16x8 {
    const char* p = cb + vo + st * 16 * ROWB;
    const s16x4 lo = __builtin_amdgcn_ds_read_tr16_b64_v4i16((lds_s16x4_ptr)(p));
    const s16x4 hi = __builtin_amdgcn_ds_read_tr16_b64_v4i16((lds_s16x4_ptr)(p + 8 * ROWB));
    return __builtin_shufflevector(lo, hi, 0, 1, 2, 3, 4, 5, 6, 7);
  };

  f32x16 o = zero16;
  float mrun = 0.f;
  auto set_m = [&](float m) {
    const float mh = bfr(m), ml = bfr(m - mh);
    mrun = mh + ml;
    const unsigned mm = pk2(-mh, -ml);
    if (hf == 0) { if (odd) q0u[0] = mm; else q1u[1] = mm; }
  };
  set_m(0.f);

  auto exps = [&](const f32x16& sx, u32x4& p0, u32x4& p1) {
#pragma unroll
    for (int i = 0; i < 4; ++i) p0[i] = pk2(__builtin_amdgcn_exp2f(sx[2 * i]), __builtin_amdgcn_exp2f(sx[2 * i + 1]));
#pragma unroll
    for (int i = 0; i < 4; ++i) p1[i] = pk2(__builtin_amdgcn_exp2f(sx[8 + 2 * i]), __builtin_amdgcn_exp2f(sx[8 + 2 * i + 1]));
  };
  auto bump = [&](f32x16& sx, bool first) -> float {
    const float mx = rowmax16(sx);
    float mq = fmaxf(mx, __shfl_xor(mx, 32)) + AT_GAP;
    if (!first) mq = fmaxf(mq, 0.f);
    const float mold = mrun;
    set_m(mold + mq);
    const float dl = mrun - mold;
    const float al = __builtin_amdgcn_exp2f(-dl);
#pragma unroll
    for (int i = 0; i < 16; ++i) { sx[i] -= dl; o[i] *= al; }
    return dl;
  };
#pragma unroll 1
  for (int ch = 0; ch < 8; ++ch) {
    if (ch + AT_NBUF - 2 < 8) wait_vm_bar<(AT_NBUF - 2) * NP>(); else wait_vm_bar<0>();
    if (ch + AT_NBUF - 1 < 8) issue(ch + AT_NBUF - 1);
    const char* cb = smem + (ch % AT_NBUF) * CH_BYTES + kg * NT * 32 * ROWB;
    f32x16 sc = qk(cb, 0);
    if (ch == 0) (void)bump(sc, true);
#pragma unroll
    for (int t = 0; t < NT; ++t) {
      f32x16 sn = zero16;
      const s16x8 vf0 = vfrag(cb, 2 * t);
      const s16x8 vf1 = vfrag(cb, 2 * t + 1);
      if (t + 1 < NT) sn = qk(cb, t + 1);
      __builtin_amdgcn_sched_barrier(0);
      u32x4 p0, p1;
      exps(sc, p0, p1);
      const unsigned any = (p0[0] | p0[1] | p0[2]) | (p0[3] | p1[0] | p1[1]) | (p1[2] | p1[3]);
      if (__any((any & 0x40004000u) != 0u)) {
        const float dl = bump(sc, false);
#pragma unroll
        for (int i = 0; i < 16; ++i) sn[i] -= dl;
        exps(sc, p0, p1);
      }
      __builtin_amdgcn_sched_barrier(0);
      o = __builtin_amdgcn_mfma_f32_32x32x16_bf16(vf0, __builtin_bit_cast(s16x8, p0), o, 0, 0, 0);
      o = __builtin_amdgcn_mfma_f32_32x32x16_bf16(vf1, __builtin_bit_cast(s16x8, p1), o, 0, 0, 0);
      __builtin_amdgcn_sched_barrier(0);
      sc = sn;
    }
  }
  if (KG == 2) {
    __syncthreads();
    float* mb = (float*)smem + w * (17 * 64);
    if (kg == 1) {
#pragma unroll
      for (int i = 0; i < 16; ++i) mb[i * 64 + lane] = o[i];
      mb[16 * 64 + lane] = mrun;
    }
    __syncthreads();
    if (kg == 1) return;
    const float m1 = mb[16 * 64 + lane];
    const float mx = fmaxf(mrun, m1);
    const float a0 = __builtin_amdgcn_exp2f(mrun - mx), a1 = __builtin_amdgcn_exp2f(m1 - mx);
#pragma unroll
    for (int i = 0; i < 16; ++i) o[i] = o[i] * a0 + mb[i * 64 + lane] * a1;
  }
  float l = odd ? o[0] : o[10];
  l = __shfl(l, r);
  const float inv = 1.0f / l;
  const int c0 = head * DH_ + 4 * hf - 2 * odd;
  if (!(odd && hf == 0)) *(unsigned*)(obuf + atile_off(qrow, c0 + 0, D_ / 32)) = pk2(o[0] * inv, o[1] * inv);
  *(unsigned*)(obuf + atile_off(qrow, c0 + 2, D_ / 32)) = pk2(o[2] * inv, o[3] * inv);
  *(unsigned*)(obuf + atile_off(qrow, c0 + 8, D_ / 32)) = pk2(o[4] * inv, o[5] * inv);
  *(unsigned*)(obuf + atile_off(qrow, c0 + 10, D_ / 32)) = pk2(o[6] * inv, o[7] * inv);
  if (hf == 0) {
    *(unsigned*)(obuf + atile_off(qrow, c0 + 16, D_ / 32)) = pk2(o[8] * inv, o[9] * inv);
    if (odd) *(unsigned*)(obuf + atile_off(qrow, c0 + 18, D_ / 32)) = pk2(o[10] * inv, o[11] * inv);
  }
}

constexpr int DSPLIT = 4;
typedef Gemm<2, 4, 4, 3, 3, 4, 4, false, 0, false> G2;
DI void down_block(char* smem, int b, const char* __restrict__ hbuf, const u16* __restrict__ w2,
                   float* __restrict__ part) {
  const int xcd = b & 7, idx = b >> 3;
  const int split = xcd >> 1, panel = (xcd & 1) * 8 + idx / 3, cg = idx % 3;
  const int c0 = (72 * split) / DSPLIT, c1 = (72 * (split + 1)) / DSPLIT;
  const int m0 = panel * 128, n0 = cg * 192, k0 = c0 * 64;
  const int w = __builtin_amdgcn_readfirstlane(threadIdx.x >> 6);
  const int wr = w >> 2, wc = w & 3;
  auto winptr = [&](int g, int& ld) -> const u16* { ld = D_; return w2 + (size_t)k0 * D_ + n0 + 64 * g; };
  auto tile = [&](int c) -> int { return wc * 3 + c; };
  f32x4 acc[4][3];
  G2::run(smem, hbuf + ((size_t)(m0 >> 4) * (F_ / 32) + (k0 >> 5)) * 1024, (size_t)(F_ / 32) * 1024, 2 * (c1 - c0), winptr, tile,
          [](int) { return true; }, acc);
  const int lane = threadIdx.x & 63, g = lane >> 4;
#pragma unroll
  for (int rb = 0; rb < 4; ++rb) {
    const int row = m0 + 64 * wr + 16 * rb + (lane & 15);
    float* pr = part + ((size_t)split * S_ + row) * D_ + n0 + 48 * wc + 4 * g;
#pragma unroll
    for (int c = 0; c < 3; ++c) *(f32x4*)(pr + 16 * c) = acc[rb][c];
  }
}

constexpr int MID_LDS = AT_LDS > G2::LDS ? AT_LDS : G2::LDS;
__global__ __launch_bounds__(512, 4) void k_mid(const char* __restrict__ qbuf, const char* __restrict__ img,
                                                char* __restrict__ obuf, const char* __restrict__ hbuf,
                                                const u16* __restrict__ w2, float* __restrict__ part) {
  extern __shared__ __attribute__((aligned(16))) char smem[];
  if (blockIdx.x < 256) attn_block<1, 3>(smem, blockIdx.x, qbuf, img, obuf);
  else down_block(smem, blockIdx.x - 256, hbuf, w2, part);
}

__global__ __launch_bounds__(1024, 4) void k_attn2(const char* __restrict__ qbuf, const char* __restrict__ img,
                                                   char* __restrict__ obuf) {
  extern __shared__ __attribute__((aligned(16))) char smem[];
  attn_block<2, 6>(smem, blockIdx.x, qbuf, img, obuf);
}

constexpr int FIN_LDS = 9 * 8192;
DI void gload16g(f32x4& dst, const void* p) { asm volatile("global_load_dwordx4 %0, %1, off" : "=v"(dst) : "v"(p) : "memory"); }
DI void gload8g(u32x2& dst, const void* p) { asm volatile("global_load_dwordx2 %0, %1, off" : "=v"(dst) : "v"(p) : "memory"); }

__global__ __launch_bounds__(256, 2) void k_final(const char* __restrict__ obuf, const u16* __restrict__ wo,
                                                  const float* __restrict__ part, const u16* __restrict__ x,
                                                  u16* __restrict__ out) {
  extern __shared__ __attribute__((aligned(16))) char smem[];
  const int b = blockIdx.x;
  const int cg = (b >> 3) % 9, panel = (b & 7) * 4 + (b >> 3) / 9;
  const int m0 = panel * 64, n0 = cg * 64;
  const int lane = threadIdx.x & 63, g = lane >> 4;
  const int w = __builtin_amdgcn_readfirstlane(threadIdx.x >> 6);
  const int wr = w >> 1, wc = w & 1;
  const unsigned lds0 = lds_base(smem);
  const int brow = lane >> 3, bip = (lane & 7) >> 1, bhalf = lane & 1;
  const unsigned vob = (unsigned)(brow * D_ + 16 * (bip ^ (((brow >> 1) & 1) | ((w & 1) << 1))) + 8 * bhalf) * 2u;
  const unsigned voa = (unsigned)lane * 16u;
  const char* bsrc = (const char*)(wo + (size_t)(w * 8) * D_ + n0);
  const char* asrc = obuf + ((size_t)(m0 >> 4) + w) * (D_ / 32) * 1024;
  auto issue = [&](int s) {
    const unsigned dst = lds0 + (unsigned)((s % 9) * 8192 + w * 1024);
    glds16s(bsrc + (size_t)s * 32 * D_ * 2, vob, dst);
    glds16s(asrc + (size_t)s * 1024, voa, dst + 4096);
  };
#pragma unroll
  for (int s = 0; s < 6; ++s) issue(s);
  f32x4 pf[2][2][DSPLIT];
  u32x2 xv[2][2];
#pragma unroll
  for (int rb = 0; rb < 2; ++rb)
#pragma unroll
    for (int c = 0; c < 2; ++c) {
      const int row = m0 + 32 * wr + 16 * rb + (lane & 15), col = n0 + 32 * wc + 16 * c + 4 * g;
#pragma unroll
      for (int sp = 0; sp < DSPLIT; ++sp) gload16g(pf[rb][c][sp], part + ((size_t)sp * S_ + row) * D_ + col);
      gload8g(xv[rb][c], x + (size_t)row * D_ + col);
    }
  constexpr int NEX = 4 * (DSPLIT + 1);
  const int q = (lane & 15) >> 2, p4 = lane & 3;
  const int bswz = ((q >> 1) & 1) | ((g & 1) << 1);
  int bo[2];
#pragma unroll
  for (int c = 0; c < 2; ++c) bo[c] = g * 1024 + q * 128 + (((wc * 2 + c) ^ bswz) * 32) + p4 * 8;
  const int ar = lane & 15;
  const int aoff = 4096 + wr * 2048 + ar * 64 + ((g ^ ((-(ar >> 2)) & 3)) * 16);
  f32x4 acc[2][2];
#pragma unroll
  for (int rb = 0; rb < 2; ++rb)
#pragma unroll
    for (int c = 0; c < 2; ++c) acc[rb][c] = f32x4{0.f, 0.f, 0.f, 0.f};
#pragma unroll
  for (int gi = 0; gi < 6; ++gi) {
    if (gi == 0) wait_vm_bar<6 + NEX>(); else if (gi < 5) wait_vm_bar<6>(); else wait_vm_bar<0>();
    if (gi < 4) { issue(3 * gi + 6); issue(3 * gi + 7); issue(3 * gi + 8); }
#pragma unroll
    for (int u = 0; u < 3; ++u) {
      const char* sb = smem + ((3 * gi + u) % 9) * 8192;
      s16x8 bf[2], af[2];
#pragma unroll
      for (int c = 0; c < 2; ++c) {
        const s16x4 b0 = __builtin_amdgcn_ds_read_tr16_b64_v4i16((lds_s16x4_ptr)(sb + bo[c]));
        const s16x4 b1 = __builtin_amdgcn_ds_read_tr16_b64_v4i16((lds_s16x4_ptr)(sb + bo[c] + 512));
        bf[c] = __builtin_shufflevector(b0, b1, 0, 1, 2, 3, 4, 5, 6, 7);
      }
#pragma unroll
      for (int rb = 0; rb < 2; ++rb) af[rb] = *(const s16x8*)(sb + aoff + rb * 1024);
#pragma unroll
      for (int rb = 0; rb < 2; ++rb)
#pragma unroll
        for (int c = 0; c < 2; ++c)
          acc[rb][c] = __builtin_amdgcn_mfma_f32_16x16x32_bf16(bf[c], af[rb], acc[rb][c], 0, 0, 0);
    }
  }
#pragma unroll
  for (int rb = 0; rb < 2; ++rb)
#pragma unroll
    for (int c = 0; c < 2; ++c) {
#pragma unroll
      for (int sp = 0; sp < DSPLIT; ++sp) asm volatile("" : "+v"(pf[rb][c][sp]));
      asm volatile("" : "+v"(xv[rb][c]));
    }
#pragma unroll
  for (int rb = 0; rb < 2; ++rb) {
#pragma unroll
    for (int c = 0; c < 2; ++c) {
      const int row = m0 + 32 * wr + 16 * rb + (lane & 15), col = n0 + 32 * wc + 16 * c + 4 * g;
      f32x4 f = pf[rb][c][0];
#pragma unroll
      for (int sp = 1; sp < DSPLIT; ++sp) f += pf[rb][c][sp];
      const u32x2 xw = xv[rb][c];
      float xf[4] = {__uint_as_float(xw[0] << 16), __uint_as_float(xw[0] & 0xffff0000u),
                     __uint_as_float(xw[1] << 16), __uint_as_float(xw[1] & 0xffff0000u)};
      float r4[4];
#pragma unroll
      for (int i = 0; i < 4; ++i) r4[i] = bfr(xf[i] + bfr(acc[rb][c][i])) + bfr(f[i]);
      u32x2 o = {pk2(r4[0], r4[1]), pk2(r4[2], r4[3])};
      *(u32x2*)(out + (size_t)row * D_ + col) = o;
    }
  }
}

constexpr size_t WS_XN = 0;
constexpr size_t WS_H = WS_XN + (size_t)S_ * D_ * 2;
constexpr size_t WS_Q = WS_H + (size_t)S_ * F_ * 2;
constexpr size_t WS_IMG = WS_Q + (size_t)H_ * S_ * ROWB;
constexpr size_t WS_O = WS_IMG + (size_t)H_ * IMG_HEAD;
constexpr size_t WS_P = WS_O + (size_t)S_ * D_ * 2;
constexpr size_t WS_END = WS_P + (size_t)DSPLIT * S_ * D_ * 4;

extern "C" void kernel_launch(void* const* d_in, const int* in_sizes, int n_in, void* d_out, int out_size,
                              void* d_ws, size_t ws_size, hipStream_t stream) {
  const u16* x = (const u16*)d_in[0];
  const u16* wqkv = (const u16*)d_in[1];
  const u16* wo = (const u16*)d_in[2];
  const u16* w1 = (const u16*)d_in[4];
  const u16* w3 = (const u16*)d_in[5];
  const u16* w2 = (const u16*)d_in[6];
  char* ws = (char*)d_ws;
  char* xn = ws + WS_XN;
  char* hbuf = ws + WS_H;
  u16* qbuf = (u16*)(ws + WS_Q);
  char* img = ws + WS_IMG;
  char* obuf = ws + WS_O;
  float* part = (float*)(ws + WS_P);
  u16* out = (u16*)d_out;

  (void)hipFuncSetAttribute((const void*)k_gemm1, hipFuncAttributeMaxDynamicSharedMemorySize, G1::LDS);
  (void)hipFuncSetAttribute((const void*)k_mid, hipFuncAttributeMaxDynamicSharedMemorySize, MID_LDS);
  (void)hipFuncSetAttribute((const void*)k_final, hipFuncAttributeMaxDynamicSharedMemorySize, FIN_LDS);

  k_ln<<<S_ / 4, 256, 0, stream>>>(x, xn);
  k_gemm1<<<256, 512, G1::LDS, stream>>>(xn, wqkv, w1, w3, hbuf, qbuf, img);
  k_mid<<<256 + 16 * 3 * DSPLIT, 512, MID_LDS, stream>>>((const char*)qbuf, img, obuf, hbuf, w2, part);
  k_final<<<32 * 9, 256, FIN_LDS, stream>>>(obuf, wo, part, x, out);
}
```

```cpp
#include <hip/hip_runtime.h>
#include <hip/hip_bf16.h>
#include <stdint.h>

typedef __attribute__((ext_vector_type(4))) short s16x4;
typedef __attribute__((ext_vector_type(8))) short s16x8;
typedef __attribute__((ext_vector_type(4))) float f32x4;
typedef __attribute__((ext_vector_type(16))) float f32x16;
typedef __attribute__((ext_vector_type(2))) unsigned u32x2;
typedef __attribute__((ext_vector_type(4))) unsigned u32x4;
typedef __attribute__((ext_vector_type(2))) __bf16 bf16x2;
typedef unsigned short u16;
typedef __attribute__((address_space(3))) s16x4* lds_s16x4_ptr;
typedef __attribute__((address_space(3))) char* lds_char_ptr;

constexpr int S_ = 2048, D_ = 576, H_ = 32, DH_ = 18, F_ = 4608, QKV_ = 1728;

#define DI __device__ __forceinline__

DI float bf2f(u16 v) { return __uint_as_float(((unsigned)v) << 16); }
DI u16 f2bf(float f) { return __builtin_bit_cast(u16, (__bf16)f); }
DI float bfr(float f) { return bf2f(f2bf(f)); }
DI unsigned pk2(float a, float b) { bf16x2 v = {(__bf16)a, (__bf16)b}; return __builtin_bit_cast(unsigned, v); }

DI void glds16(const void* gsrc, unsigned lds_dst) {
  unsigned keep;
  asm volatile("s_mov_b32 %0, m0\n\ts_mov_b32 m0, %2\n\ts_nop 0\n\tglobal_load_lds_dwordx4 %1, off\n\ts_mov_b32 m0, %0"
               : "=&s"(keep) : "v"(gsrc), "s"(lds_dst) : "memory");
}
DI void glds16s(const void* sbase, unsigned voff, unsigned lds_dst) {
  unsigned keep;
  asm volatile("s_mov_b32 %0, m0\n\ts_mov_b32 m0, %3\n\ts_nop 0\n\tglobal_load_lds_dwordx4 %1, %2\n\ts_mov_b32 m0, %0"
               : "=&s"(keep) : "v"(voff), "s"(sbase), "s"(lds_dst) : "memory");
}
template <int N> DI void wait_vm_bar() { asm volatile("s_waitcnt vmcnt(%0)\n\ts_barrier" ::"i"(N) : "memory"); }

DI unsigned lds_base(char* smem) { return (unsigned)(uintptr_t)(lds_char_ptr)smem; }

DI size_t atile_off(int row, int k, int nks) {
  const int r = row & 15, ch = (k >> 3) & 3;
  return ((size_t)(row >> 4) * nks + (k >> 5)) * 1024 + r * 64 + ((ch ^ ((-(r >> 2)) & 3)) << 4) + (k & 7) * 2;
}

__global__ __launch_bounds__(256) void k_ln(const u16* __restrict__ x, char* __restrict__ xn,
                                            const u16* __restrict__ wqkv, const u16* __restrict__ w1,
                                            const u16* __restrict__ w3, const u16* __restrict__ w2,
                                            const u16* __restrict__ wo) {
  if (blockIdx.x >= S_ / 4) {
    const int b = blockIdx.x - S_ / 4, xg = b & 7, part = b >> 3;
    unsigned acc = 0;
    for (int i = threadIdx.x; i < 18 * 9; i += 256) {
      const int k = part * 18 + i / 9, ln = i % 9;
      acc ^= *(const unsigned*)(w1 + (size_t)k * F_ + xg * 576 + ln * 64);
      acc ^= *(const unsigned*)(w3 + (size_t)k * F_ + xg * 576 + ln * 64);
    }
    for (int i = threadIdx.x; i < 18 * 4; i += 256) {
      const int k = part * 18 + i / 4, c = (xg * 216 & ~63) + (i % 4) * 64;
      acc ^= *(const unsigned*)(wqkv + (size_t)k * QKV_ + (c < QKV_ ? c : 0));
    }
    for (int i = threadIdx.x; i < 18 * 9; i += 256) {
      const int k = b * 18 + i / 9, ln = i % 9;
      acc ^= *(const unsigned*)(w2 + (size_t)k * D_ + ln * 64);
      if (k < D_) acc ^= *(const unsigned*)(wo + (size_t)k * D_ + ln * 64);
    }
    asm volatile("" :: "v"(acc));
    return;
  }
  const int row = blockIdx.x * 4 + (threadIdx.x >> 6), lane = threadIdx.x & 63;
  const u16* xr = x + (size_t)row * D_;
  u32x4 c0 = *(const u32x4*)(xr + lane * 8);
  u32x4 c1 = {0u, 0u, 0u, 0u};
  const bool two = lane < 8;
  if (two) c1 = *(const u32x4*)(xr + 512 + lane * 8);
  float v[16];
#pragma unroll
  for (int i = 0; i < 4; ++i) {
    v[2 * i] = __uint_as_float(c0[i] << 16); v[2 * i + 1] = __uint_as_float(c0[i] & 0xffff0000u);
    v[8 + 2 * i] = __uint_as_float(c1[i] << 16); v[8 + 2 * i + 1] = __uint_as_float(c1[i] & 0xffff0000u);
  }
  float s = 0.f;
#pragma unroll
  for (int i = 0; i < 16; ++i) s += v[i];
#pragma unroll
  for (int o = 32; o > 0; o >>= 1) s += __shfl_xor(s, o);
  const float mean = s * (1.0f / D_);
  float q = 0.f;
#pragma unroll
  for (int i = 0; i < 8; ++i) { float d = v[i] - mean; q += d * d; }
  if (two) {
#pragma unroll
    for (int i = 8; i < 16; ++i) { float d = v[i] - mean; q += d * d; }
  }
#pragma unroll
  for (int o = 32; o > 0; o >>= 1) q += __shfl_xor(q, o);
  const float var = bfr(q * (1.0f / D_));
  const float m = bfr(mean);
  const float sd = bfr(sqrtf(bfr(var + 1e-5f)));
  u32x4 o0, o1;
#pragma unroll
  for (int i = 0; i < 4; ++i) {
    o0[i] = pk2(bfr(v[2 * i] - m) / sd, bfr(v[2 * i + 1] - m) / sd);
    o1[i] = pk2(bfr(v[8 + 2 * i] - m) / sd, bfr(v[8 + 2 * i + 1] - m) / sd);
  }
  *(u32x4*)(xn + atile_off(row, lane * 8, D_ / 32)) = o0;
  if (two) *(u32x4*)(xn + atile_off(row, 512 + lane * 8, D_ / 32)) = o1;
}

template <int WR, int WC, int RB, int NTW, int NG, int NSTAGE, int LW, bool XPF, int PFD, bool XL>
struct Gemm {
  static constexpr int NW = WR * WC;
  static constexpr int ROWS = WR * RB * 16;
  static constexpr int BSTEP = 4 * NG * 1024;
  static constexpr int ASTEP = ROWS * 64;
  static constexpr int STAGE = BSTEP + ASTEP;
  static constexpr int LDS = STAGE * NSTAGE;
  static constexpr int NBT = 4 * NG, NAT = ROWS / 16, NI = NBT + NAT;
  static constexpr int NGL = (NI + LW - 1) / LW;
  static constexpr int AHEAD = XPF ? 1 : 0;
  static_assert(NSTAGE >= 2 + AHEAD, "ring too short");
  static constexpr int TW = NW - LW;
  static constexpr int NTI = PFD > 0 ? (NI * 8 + 64 * TW - 1) / (64 * TW) : 0;

  template <typename WinFn, typename TileFn, typename UseFn>
  static DI void run(char* smem, const char* __restrict__ A, size_t rbs, int nk, WinFn winptr, TileFn tile, UseFn used,
                     f32x4 (&acc)[RB][NTW]) {
    const int tid = threadIdx.x, lane = tid & 63;
    const int w = __builtin_amdgcn_readfirstlane(tid >> 6);
    const int wr = w / WC;
    const bool loader = XL ? w >= NW : w < LW;
    const int lw = XL ? (w >= NW ? w - NW : 0) : w % LW;
    const unsigned lds0 = lds_base(smem);
    const char* src[NGL]; int sstep[NGL]; unsigned sdst[NGL];
    unsigned vob[2][2];
    unsigned onmask = 0;
    int kind[NGL];
    const int brow = lane >> 3, bip = (lane & 7) >> 1, bhalf = lane & 1;
    int ld0 = 0;
    (void)winptr(0, ld0);
    int ld1 = ld0;
    (void)winptr(NG - 1, ld1);
#pragma unroll
    for (int kp = 0; kp < 2; ++kp) {
      const int swz = ((brow >> 1) & 1) | (kp << 1);
      vob[kp][0] = (unsigned)(brow * ld0 + 16 * (bip ^ swz) + 8 * bhalf) * 2u;
      vob[kp][1] = (unsigned)(brow * ld1 + 16 * (bip ^ swz) + 8 * bhalf) * 2u;
    }
    const unsigned voa = (unsigned)lane * 16u;
#pragma unroll
    for (int j = 0; j < NGL; ++j) {
      int id = lw + LW * j;
      if (id >= NI) id -= NI;
      if (id < NBT) {
        const int kb = id / NG, grp = id % NG;
        const int swz = ((brow >> 1) & 1) | ((kb & 1) << 1);
        int ld;
        const u16* p = winptr(grp, ld);
        src[j] = (const char*)(p + (size_t)(kb * 8) * ld);
        kind[j] = (kb & 1) * 2 + (ld == ld0 ? 0 : 1);
        if (used(grp * 4 + (bip ^ swz)) || lane == 0) onmask |= 1u << j;
        sstep[j] = 32 * ld * 2;
        sdst[j] = (unsigned)id * 1024u;
      } else {
        const int rb = id - NBT;
        src[j] = A + rb * rbs;
        kind[j] = 4;
        onmask |= 1u << j;
        sstep[j] = 1024;
        sdst[j] = (unsigned)(BSTEP + rb * 1024);
      }
    }
    const char* tp[NTI > 0 ? NTI : 1]; int tstep[NTI > 0 ? NTI : 1];
    if constexpr (PFD > 0) {
#pragma unroll
      for (int jt = 0; jt < NTI; ++jt) {
        int id = ((w - LW + TW) % TW + TW * jt) * 8 + (lane >> 3);
        id = id < NI ? id : NI - 1;
        const int ln = lane & 7;
        if (id < NBT) {
          int ld;
          const u16* p = winptr(id % NG, ld);
          tp[jt] = (const char*)(p + (size_t)((id / NG) * 8 + ln) * ld);
          tstep[jt] = 32 * ld * 2;
        } else {
          tp[jt] = A + (id - NBT) * rbs + ln * 128;
          tstep[jt] = 1024;
        }
        tp[jt] += (size_t)(NSTAGE - 1) * tstep[jt];
      }
    }
    unsigned sink = 0;
    auto touch = [&]() {
#pragma unroll
      for (int jt = 0; jt < NTI; ++jt) {
        asm volatile("global_load_dword %0, %1, off" : "+v"(sink) : "v"(tp[jt]) : "memory");
        tp[jt] += tstep[jt];
      }
    };
    unsigned ibuf = 0;
    auto issue1 = [&](int j) {
      const int kd = kind[j];
      const unsigned vo = kd == 4 ? voa : kd == 0 ? vob[0][0] : kd == 1 ? vob[0][1] : kd == 2 ? vob[1][0] : vob[1][1];
      if ((onmask >> j) & 1u) glds16s(src[j], vo, lds0 + ibuf + sdst[j]);
      src[j] += sstep[j];
    };
    auto issue_done = [&]() { ibuf = ibuf + STAGE == LDS ? 0u : ibuf + STAGE; };
    const int g = lane >> 4, q = (lane & 15) >> 2, p4 = lane & 3;
    const int bswz = ((q >> 1) & 1) | ((g & 1) << 1);
    int bo[NTW];
#pragma unroll
    for (int c = 0; c < NTW; ++c) {
      const int t = tile(c);
      bo[c] = g * NG * 1024 + q * 128 + (((t & 3) ^ bswz) * 32) + p4 * 8 + (t >> 2) * 1024;
    }
    const int ar = lane & 15;
    const int aoff = BSTEP + wr * RB * 1024 + ar * 64 + ((g ^ ((-(ar >> 2)) & 3)) * 16);

#pragma unroll
    for (int rb = 0; rb < RB; ++rb)
#pragma unroll
      for (int c = 0; c < NTW; ++c) acc[rb][c] = f32x4{0.f, 0.f, 0.f, 0.f};

    auto ldb = [&](const char* sbk, int c) -> s16x8 {
      const char* bp = sbk + bo[c];
      const s16x4 b0 = __builtin_amdgcn_ds_read_tr16_b64_v4i16((lds_s16x4_ptr)(bp));
      const s16x4 b1 = __builtin_amdgcn_ds_read_tr16_b64_v4i16((lds_s16x4_ptr)(bp + 512));
      return __builtin_shufflevector(b0, b1, 0, 1, 2, 3, 4, 5, 6, 7);
    };
    auto lda = [&](const char* sbk, s16x8 (&dst)[RB]) {
#pragma unroll
      for (int rb = 0; rb < RB; ++rb) dst[rb] = *(const s16x8*)(sbk + aoff + rb * 1024);
    };

    if (loader) {
#pragma unroll
      for (int s = 0; s < NSTAGE - 1; ++s) {
#pragma unroll
        for (int j = 0; j < NGL; ++j) issue1(j);
        issue_done();
      }
    } else if constexpr (PFD > 0) {
      for (int s = NSTAGE - 1; s < NSTAGE - 1 + PFD && s < nk; ++s) touch();
    }
    wait_vm_bar<(NSTAGE - 2 - AHEAD) * NGL>();
    if (XL && loader) {
#pragma unroll 1
      for (int s = 0; s + 1 < nk; ++s) {
        if (s + NSTAGE - 1 < nk) {
#pragma unroll
          for (int j = 0; j < NGL; ++j) issue1(j);
          issue_done();
          wait_vm_bar<(NSTAGE - 2 - AHEAD) * NGL>();
        } else wait_vm_bar<0>();
      }
      return;
    }
    s16x8 a[2][RB];
    s16x8 bA = ldb(smem, 0);
    lda(smem, a[0]);
    s16x8 bB = ldb(smem, NTW > 1 ? 1 : 0);

    unsigned cbuf = 0;
#pragma unroll 1
    for (int s0 = 0; s0 < nk; s0 += 2) {
#pragma unroll
      for (int u = 0; u < 2; ++u) {
        const int s = s0 + u;
        if (s >= nk) break;
        const bool do_issue = loader && s + NSTAGE - 1 < nk, has_next = s + 1 < nk;
        const char* sb = smem + cbuf;
        cbuf = cbuf + STAGE == LDS ? 0u : cbuf + STAGE;
        const char* nb = smem + cbuf;
        if (do_issue) {
#pragma unroll
          for (int j = 0; j < NGL; ++j) issue1(j);
          issue_done();
        }
        if constexpr (PFD > 0) { if (!loader && s + NSTAGE - 1 + PFD < nk) touch(); }
        s16x8 bq[NTW + 2];
        bq[0] = bA; bq[1] = bB;
#pragma unroll
        for (int c = 0; c < NTW; ++c) {
          if (c + 2 < NTW) bq[c + 2] = ldb(sb, c + 2);
          else {
            bq[c + 2] = bq[c];
            if (XPF && has_next) {
              if (c + 2 == NTW || NTW == 1) lda(nb, a[u ^ 1]);
              bq[c + 2] = ldb(nb, (c + 2 - NTW) % NTW);
            }
          }
          __builtin_amdgcn_sched_barrier(0);
#pragma unroll
          for (int rb = 0; rb < RB; ++rb)
            acc[rb][c] = __builtin_amdgcn_mfma_f32_16x16x32_bf16(bq[c], a[u][rb], acc[rb][c], 0, 0, 0);
          __builtin_amdgcn_sched_barrier(0);
        }
        bA = bq[NTW]; bB = bq[NTW + 1];
        if (has_next) {
          if (s + NSTAGE - 1 < nk) wait_vm_bar<(NSTAGE - 2 - AHEAD) * NGL>(); else wait_vm_bar<0>();
          if (!XPF) {
            bA = ldb(nb, 0);
            lda(nb, a[u ^ 1]);
            bB = ldb(nb, NTW > 1 ? 1 : 0);
          }
        }
      }
    }
    if constexpr (PFD > 0) asm volatile("s_waitcnt vmcnt(0)" : "+v"(sink) : : "memory");
  }
};

constexpr int ROWB = 48;
constexpr int CH_K = 0, CH_V = 256 * ROWB, CH_BYTES = 512 * ROWB;
constexpr int IMG_HEAD = 8 * CH_BYTES;

typedef Gemm<4, 2, 4, 11, 8, 3, 4, true, 0, false> G1;
constexpr float QSCALE = 0.23570226039551584f * 1.4426950408889634f;

DI float silu_mul(float gt, float up) {
  const float e = __builtin_amdgcn_exp2f(gt * -1.4426950408889634f);
  return gt * __builtin_amdgcn_rcpf(1.0f + e) * up;
}

__global__ __launch_bounds__(512, 2) void k_gemm1(const char* __restrict__ xn, const u16* __restrict__ wqkv,
                                                  const u16* __restrict__ w1, const u16* __restrict__ w3,
                                                  char* __restrict__ hbuf, u16* __restrict__ qbuf,
                                                  char* __restrict__ img) {
  extern __shared__ __attribute__((aligned(16))) char smem[];
  const int b = blockIdx.x;
  const int cgp = (b & 7) * 4 + ((b >> 3) & 3);
  const int panel = b >> 5;
  const int m0 = panel * 256, n0 = cgp * 144;
  const int qt0 = (108 * cgp) >> 5, qt1 = (108 * (cgp + 1)) >> 5;
  const int aw = n0 & ~63, toff = (n0 & 63) >> 4;
  const int qaw = (16 * qt0) & ~63, qoff = qt0 & 3;
  const int w = __builtin_amdgcn_readfirstlane(threadIdx.x >> 6);
  const int wr = w >> 1, wc = w & 1;
  auto winptr = [&](int g, int& ld) -> const u16* {
    if (g < 3) { ld = F_; return w1 + aw + 64 * g; }
    if (g < 6) { ld = F_; return w3 + aw + 64 * (g - 3); }
    ld = QKV_;
    const int c = qaw + 64 * (g - 6);
    return wqkv + (c < QKV_ ? c : qaw);
  };
  auto tile = [&](int c) -> int {
    if (wc == 0) return c < 5 ? toff + c : c < 10 ? 12 + toff + (c - 5) : 24 + qoff;
    const int t = c < 4 ? toff + 5 + c : c < 8 ? 12 + toff + 1 + c : 24 + qoff + 1 + (c - 8);
    return t < 32 ? t : 31;
  };
  auto used = [&](int t) -> bool {
    return t < 12 ? (unsigned)(t - toff) < 9u : t < 24 ? (unsigned)(t - 12 - toff) < 9u : (unsigned)(t - 24 - qoff) < (unsigned)(qt1 - qt0);
  };
  f32x4 acc[4][11];
  G1::run(smem, xn + (size_t)(m0 >> 4) * (D_ / 32) * 1024, (size_t)(D_ / 32) * 1024, D_ / 32, winptr, tile, used, acc);

  const int lane = threadIdx.x & 63, g = lane >> 4;
  const int row0 = m0 + 64 * wr + (lane & 15);
  if (wc == 0) {
#pragma unroll
    for (int c = 0; c < 5; ++c) {
      char* hp = hbuf + atile_off(row0, n0 + 16 * c + 4 * g, F_ / 32);
#pragma unroll
      for (int rb = 0; rb < 4; ++rb) {
        u32x2 o = {pk2(silu_mul(acc[rb][c][0], acc[rb][c + 5][0]), silu_mul(acc[rb][c][1], acc[rb][c + 5][1])),
                   pk2(silu_mul(acc[rb][c][2], acc[rb][c + 5][2]), silu_mul(acc[rb][c][3], acc[rb][c + 5][3]))};
        *(u32x2*)(hp + (size_t)rb * (F_ / 32) * 1024) = o;
      }
      __builtin_amdgcn_sched_barrier(0);
    }
  } else {
#pragma unroll
    for (int c = 0; c < 4; ++c) {
      char* hp = hbuf + atile_off(row0, n0 + 80 + 16 * c + 4 * g, F_ / 32);
#pragma unroll
      for (int rb = 0; rb < 4; ++rb) {
        u32x2 o = {pk2(silu_mul(acc[rb][c][0], acc[rb][c + 4][0]), silu_mul(acc[rb][c][1], acc[rb][c + 4][1])),
                   pk2(silu_mul(acc[rb][c][2], acc[rb][c + 4][2]), silu_mul(acc[rb][c][3], acc[rb][c + 4][3]))};
        *(u32x2*)(hp + (size_t)rb * (F_ / 32) * 1024) = o;
      }
      __builtin_amdgcn_sched_barrier(0);
    }
  }
#pragma unroll
  for (int c = 8; c < 11; ++c) {
    int t;
    if (wc == 0) { if (c != 10) continue; t = qt0; } else t = qt0 + 1 + (c - 8);
    if (t >= qt1) continue;
    const int which = t / 36;
    const int cc0 = 16 * (t - 36 * which) + 4 * g;
    const int head = cc0 / DH_, d0 = cc0 - head * DH_;
    const bool strad = d0 == 16;
    const int pos = d0 + 2 * (head & 1);
    const float sc = which == 0 ? QSCALE : 1.0f;
    char* base;
    size_t hstride;
    if (which == 0) { base = (char*)qbuf + (size_t)row0 * ROWB; hstride = (size_t)S_ * ROWB; }
    else { base = img + (row0 >> 8) * CH_BYTES + (which == 1 ? CH_K : CH_V) + (row0 & 255) * ROWB; hstride = IMG_HEAD; }
    char* p = base + head * hstride + pos * 2;
#pragma unroll
    for (int rb = 0; rb < 4; ++rb) {
      const unsigned lo = pk2(acc[rb][c][0] * sc, acc[rb][c][1] * sc), hi = pk2(acc[rb][c][2] * sc, acc[rb][c][3] * sc);
      u32x2 v = {lo, strad ? 0x3f803f80u : hi};
      *(u32x2*)(p + rb * 16 * ROWB) = v;
      if (strad) { u32x2 v2 = {0x3f803f80u, hi}; *(u32x2*)(base + (head + 1) * hstride + rb * 16 * ROWB) = v2; }
    }
  }
}

constexpr int AT_LDS = 3 * CH_BYTES;
constexpr float AT_GAP = 7.0f;

DI float max3f(float a, float b, float c) { return fmaxf(fmaxf(a, b), c); }
DI float rowmax16(const f32x16& s) {
  float mx = max3f(s[0], s[1], s[2]);
#pragma unroll
  for (int i = 3; i < 15; i += 2) mx = max3f(mx, s[i], s[i + 1]);
  return fmaxf(mx, s[15]);
}

template <int KG, int AT_NBUF>
DI void attn_block(char* smem, int ab, const char* __restrict__ qbuf, const char* __restrict__ img,
                   char* __restrict__ obuf) {
  const int tid = threadIdx.x, lane = tid & 63, r = lane & 31, hf = lane >> 5;
  const int wv = __builtin_amdgcn_readfirstlane(tid >> 6);
  const int w = wv & 7, kg = wv >> 3;
  constexpr int NT = 8 / KG, NP = 3 / KG + (3 % KG ? 1 : 0);
  const int head = (ab & 7) * 4 + ((ab >> 3) & 3), qb = ab >> 5, odd = head & 1;
  const unsigned lds0 = lds_base(smem);
  const char* gsrc = img + (size_t)head * IMG_HEAD;
  const unsigned vl = lane * 16;
  auto issue = [&](int ch) {
    const unsigned dst = lds0 + (unsigned)((ch % AT_NBUF) * CH_BYTES);
    const char* s = gsrc + (size_t)ch * CH_BYTES;
#pragma unroll
    for (int j = 0; j < NP; ++j) {
      int pc = wv + 8 * KG * j;
      if (pc >= 24) pc -= 24;
      glds16s(s + pc * 1024, vl, dst + pc * 1024);
    }
  };
#pragma unroll
  for (int c = 0; c < AT_NBUF - 1; ++c) issue(c);

  const int qrow = qb * 256 + w * 32 + r;
  const char* qp = qbuf + ((size_t)head * S_ + qrow) * ROWB;
  u32x4 q0u = *(const u32x4*)(qp + 16 * hf);
  u32x4 q1u = {0u, 0u, 0u, 0u};
  if (hf == 0) { const u32x2 t2 = *(const u32x2*)(qp + 32); q1u[0] = t2[0]; q1u[1] = t2[1]; }

  const int kmo = CH_K + r * ROWB + hf * 16;
  const int kto = CH_K + r * ROWB + 32;
  const int vo = CH_V + (4 * hf + ((lane & 15) >> 2)) * ROWB + ((lane >> 4) & 1) * 32 + (lane & 3) * 8;
  const f32x16 zero16 = {0.f, 0.f, 0.f, 0.f, 0.f, 0.f, 0.f, 0.f, 0.f, 0.f, 0.f, 0.f, 0.f, 0.f, 0.f, 0.f};

  auto qk = [&](const char* cb, int t) -> f32x16 {
    const s16x8 kf0 = *(const s16x8*)(cb + kmo + t * 32 * ROWB);
    const u32x2 kt = *(const u32x2*)(cb + kto + t * 32 * ROWB);
    const u32x4 k1u = {kt[0], kt[1], 0u, 0u};
    f32x16 s = __builtin_amdgcn_mfma_f32_32x32x16_bf16(kf0, __builtin_bit_cast(s16x8, q0u), zero16, 0, 0, 0);
    return __builtin_amdgcn_mfma_f32_32x32x16_bf16(__builtin_bit_cast(s16x8, k1u), __builtin_bit_cast(s16x8, q1u), s, 0, 0, 0);
  };
  auto vfrag = [&](const char* cb, int st) -> s16x8 {
    const char* p = cb + vo + st * 16 * ROWB;
    const s16x4 lo = __builtin_amdgcn_ds_read_tr16_b64_v4i16((lds_s16x4_ptr)(p));
    const s16x4 hi = __builtin_amdgcn_ds_read_tr16_b64_v4i16((lds_s16x4_ptr)(p + 8 * ROWB));
    return __builtin_shufflevector(lo, hi, 0, 1, 2, 3, 4, 5, 6, 7);
  };

  f32x16 o = zero16;
  float mrun = 0.f;
  auto set_m = [&](float m) {
    const float mh = bfr(m), ml = bfr(m - mh);
    mrun = mh + ml;
    const unsigned mm = pk2(-mh, -ml);
    if (hf == 0) { if (odd) q0u[0] = mm; else q1u[1] = mm; }
  };
  set_m(0.f);

  auto exps = [&](const f32x16& sx, u32x4& p0, u32x4& p1) {
#pragma unroll
    for (int i = 0; i < 4; ++i) p0[i] = pk2(__builtin_amdgcn_exp2f(sx[2 * i]), __builtin_amdgcn_exp2f(sx[2 * i + 1]));
#pragma unroll
    for (int i = 0; i < 4; ++i) p1[i] = pk2(__builtin_amdgcn_exp2f(sx[8 + 2 * i]), __builtin_amdgcn_exp2f(sx[8 + 2 * i + 1]));
  };
  auto bump = [&](f32x16& sx, bool first) -> float {
    const float mx = rowmax16(sx);
    float mq = fmaxf(mx, __shfl_xor(mx, 32)) + AT_GAP;
    if (!first) mq = fmaxf(mq, 0.f);
    const float mold = mrun;
    set_m(mold + mq);
    const float dl = mrun - mold;
    const float al = __builtin_amdgcn_exp2f(-dl);
#pragma unroll
    for (int i = 0; i < 16; ++i) { sx[i] -= dl; o[i] *= al; }
    return dl;
  };
#pragma unroll 1
  for (int ch = 0; ch < 8; ++ch) {
    if (ch + AT_NBUF - 2 < 8) wait_vm_bar<(AT_NBUF - 2) * NP>(); else wait_vm_bar<0>();
    if (ch + AT_NBUF - 1 < 8) issue(ch + AT_NBUF - 1);
    const char* cb = smem + (ch % AT_NBUF) * CH_BYTES + kg * NT * 32 * ROWB;
    f32x16 sc = qk(cb, 0);
    if (ch == 0) (void)bump(sc, true);
#pragma unroll
    for (int t = 0; t < NT; ++t) {
      f32x16 sn = zero16;
      const s16x8 vf0 = vfrag(cb, 2 * t);
      const s16x8 vf1 = vfrag(cb, 2 * t + 1);
      if (t + 1 < NT) sn = qk(cb, t + 1);
      __builtin_amdgcn_sched_barrier(0);
      u32x4 p0, p1;
      exps(sc, p0, p1);
      const unsigned any = (p0[0] | p0[1] | p0[2]) | (p0[3] | p1[0] | p1[1]) | (p1[2] | p1[3]);
      if (__any((any & 0x40004000u) != 0u)) {
        const float dl = bump(sc, false);
#pragma unroll
        for (int i = 0; i < 16; ++i) sn[i] -= dl;
        exps(sc, p0, p1);
      }
      __builtin_amdgcn_sched_barrier(0);
      o = __builtin_amdgcn_mfma_f32_32x32x16_bf16(vf0, __builtin_bit_cast(s16x8, p0), o, 0, 0, 0);
      o = __builtin_amdgcn_mfma_f32_32x32x16_bf16(vf1, __builtin_bit_cast(s16x8, p1), o, 0, 0, 0);
      __builtin_amdgcn_sched_barrier(0);
      sc = sn;
    }
  }
  if (KG == 2) {
    __syncthreads();
    float* mb = (float*)smem + w * (17 * 64);
    if (kg == 1) {
#pragma unroll
      for (int i = 0; i < 16; ++i) mb[i * 64 + lane] = o[i];
      mb[16 * 64 + lane] = mrun;
    }
    __syncthreads();
    if (kg == 1) return;
    const float m1 = mb[16 * 64 + lane];
    const float mx = fmaxf(mrun, m1);
    const float a0 = __builtin_amdgcn_exp2f(mrun - mx), a1 = __builtin_amdgcn_exp2f(m1 - mx);
#pragma unroll
    for (int i = 0; i < 16; ++i) o[i] = o[i] * a0 + mb[i * 64 + lane] * a1;
  }
  float l = odd ? o[0] : o[10];
  l = __shfl(l, r);
  const float inv = 1.0f / l;
  const int c0 = head * DH_ + 4 * hf - 2 * odd;
  if (!(odd && hf == 0)) *(unsigned*)(obuf + atile_off(qrow, c0 + 0, D_ / 32)) = pk2(o[0] * inv, o[1] * inv);
  *(unsigned*)(obuf + atile_off(qrow, c0 + 2, D_ / 32)) = pk2(o[2] * inv, o[3] * inv);
  *(unsigned*)(obuf + atile_off(qrow, c0 + 8, D_ / 32)) = pk2(o[4] * inv, o[5] * inv);
  *(unsigned*)(obuf + atile_off(qrow, c0 + 10, D_ / 32)) = pk2(o[6] * inv, o[7] * inv);
  if (hf == 0) {
    *(unsigned*)(obuf + atile_off(qrow, c0 + 16, D_ / 32)) = pk2(o[8] * inv, o[9] * inv);
    if (odd) *(unsigned*)(obuf + atile_off(qrow, c0 + 18, D_ / 32)) = pk2(o[10] * inv, o[11] * inv);
  }
}

constexpr int DSPLIT = 4;
typedef Gemm<2, 4, 4, 3, 3, 4, 4, false, 0, false> G2;
DI void down_block(char* smem, int b, const char* __restrict__ hbuf, const u16* __restrict__ w2,
                   char* __restrict__ part) {
  const int xcd = b & 7, idx = b >> 3;
  const int split = xcd >> 1, panel = (xcd & 1) * 8 + idx / 3, cg = idx % 3;
  const int c0 = (72 * split) / DSPLIT, c1 = (72 * (split + 1)) / DSPLIT;
  const int m0 = panel * 128, n0 = cg * 192, k0 = c0 * 64;
  const int w = __builtin_amdgcn_readfirstlane(threadIdx.x >> 6);
  const int wr = w >> 2, wc = w & 3;
  auto winptr = [&](int g, int& ld) -> const u16* { ld = D_; return w2 + (size_t)k0 * D_ + n0 + 64 * g; };
  auto tile = [&](int c) -> int { return wc * 3 + c; };
  f32x4 acc[4][3];
  G2::run(smem, hbuf + ((size_t)(m0 >> 4) * (F_ / 32) + (k0 >> 5)) * 1024, (size_t)(F_ / 32) * 1024, 2 * (c1 - c0), winptr, tile,
          [](int) { return true; }, acc);
  const int lane = threadIdx.x & 63;
#pragma unroll
  for (int rb = 0; rb < 4; ++rb) {
    const int rblk = (m0 >> 4) + 4 * wr + rb;
    char* pr = part + (((size_t)split * (S_ / 16) + rblk) * (D_ / 16) + (n0 >> 4) + 3 * wc) * 512 + lane * 8;
#pragma unroll
    for (int c = 0; c < 3; ++c) {
      u32x2 v = {pk2(acc[rb][c][0], acc[rb][c][1]), pk2(acc[rb][c][2], acc[rb][c][3])};
      *(u32x2*)(pr + 512 * c) = v;
    }
  }
}

constexpr int MID_LDS = AT_LDS > G2::LDS ? AT_LDS : G2::LDS;
__global__ __launch_bounds__(512, 4) void k_mid(const char* __restrict__ qbuf, const char* __restrict__ img,
                                                char* __restrict__ obuf, const char* __restrict__ hbuf,
                                                const u16* __restrict__ w2, char* __restrict__ part) {
  extern __shared__ __attribute__((aligned(16))) char smem[];
  if (blockIdx.x < 256) attn_block<1, 3>(smem, blockIdx.x, qbuf, img, obuf);
  else down_block(smem, blockIdx.x - 256, hbuf, w2, part);
}

__global__ __launch_bounds__(1024, 4) void k_attn2(const char* __restrict__ qbuf, const char* __restrict__ img,
                                                   char* __restrict__ obuf) {
  extern __shared__ __attribute__((aligned(16))) char smem[];
  attn_block<2, 6>(smem, blockIdx.x, qbuf, img, obuf);
}

constexpr int FIN_LDS = 9 * 8192;
DI void gload16g(f32x4& dst, const void* p) { asm volatile("global_load_dwordx4 %0, %1, off" : "=v"(dst) : "v"(p) : "memory"); }
DI void gload8g(u32x2& dst, const void* p) { asm volatile("global_load_dwordx2 %0, %1, off" : "=v"(dst) : "v"(p) : "memory"); }

__global__ __launch_bounds__(256, 2) void k_final(const char* __restrict__ obuf, const u16* __restrict__ wo,
                                                  const char* __restrict__ part, const u16* __restrict__ x,
                                                  u16* __restrict__ out) {
  extern __shared__ __attribute__((aligned(16))) char smem[];
  const int b = blockIdx.x;
  const int cg = (b >> 3) % 9, panel = (b & 7) * 4 + (b >> 3) / 9;
  const int m0 = panel * 64, n0 = cg * 64;
  const int lane = threadIdx.x & 63, g = lane >> 4;
  const int w = __builtin_amdgcn_readfirstlane(threadIdx.x >> 6);
  const int wr = w >> 1, wc = w & 1;
  const unsigned lds0 = lds_base(smem);
  const int brow = lane >> 3, bip = (lane & 7) >> 1, bhalf = lane & 1;
  const unsigned vob = (unsigned)(brow * D_ + 16 * (bip ^ (((brow >> 1) & 1) | ((w & 1) << 1))) + 8 * bhalf) * 2u;
  const unsigned voa = (unsigned)lane * 16u;
  const char* bsrc = (const char*)(wo + (size_t)(w * 8) * D_ + n0);
  const char* asrc = obuf + ((size_t)(m0 >> 4) + w) * (D_ / 32) * 1024;
  auto issue = [&](int s) {
    const unsigned dst = lds0 + (unsigned)((s % 9) * 8192 + w * 1024);
    glds16s(bsrc + (size_t)s * 32 * D_ * 2, vob, dst);
    glds16s(asrc + (size_t)s * 1024, voa, dst + 4096);
  };
#pragma unroll
  for (int s = 0; s < 6; ++s) issue(s);
  u32x2 pf[2][2][DSPLIT];
  u32x2 xv[2][2];
#pragma unroll
  for (int rb = 0; rb < 2; ++rb)
#pragma unroll
    for (int c = 0; c < 2; ++c) {
      const int row = m0 + 32 * wr + 16 * rb + (lane & 15), col = n0 + 32 * wc + 16 * c + 4 * g;
#pragma unroll
      for (int sp = 0; sp < DSPLIT; ++sp)
        gload8g(pf[rb][c][sp], part + (((size_t)sp * (S_ / 16) + (m0 >> 4) + 2 * wr + rb) * (D_ / 16) + (n0 >> 4) + 2 * wc + c) * 512 + lane * 8);
      gload8g(xv[rb][c], x + (size_t)row * D_ + col);
    }
  constexpr int NEX = 4 * (DSPLIT + 1);
  const int q = (lane & 15) >> 2, p4 = lane & 3;
  const int bswz = ((q >> 1) & 1) | ((g & 1) << 1);
  int bo[2];
#pragma unroll
  for (int c = 0; c < 2; ++c) bo[c] = g * 1024 + q * 128 + (((wc * 2 + c) ^ bswz) * 32) + p4 * 8;
  const int ar = lane & 15;
  const int aoff = 4096 + wr * 2048 + ar * 64 + ((g ^ ((-(ar >> 2)) & 3)) * 16);
  f32x4 acc[2][2];
#pragma unroll
  for (int rb = 0; rb < 2; ++rb)
#pragma unroll
    for (int c = 0; c < 2; ++c) acc[rb][c] = f32x4{0.f, 0.f, 0.f, 0.f};
#pragma unroll
  for (int gi = 0; gi < 6; ++gi) {
    if (gi == 0) wait_vm_bar<6 + NEX>(); else if (gi < 5) wait_vm_bar<6>(); else wait_vm_bar<0>();
    if (gi < 4) { issue(3 * gi + 6); issue(3 * gi + 7); issue(3 * gi + 8); }
#pragma unroll
    for (int u = 0; u < 3; ++u) {
      const char* sb = smem + ((3 * gi + u) % 9) * 8192;
      s16x8 bf[2], af[2];
#pragma unroll
      for (int c = 0; c < 2; ++c) {
        const s16x4 b0 = __builtin_amdgcn_ds_read_tr16_b64_v4i16((lds_s16x4_ptr)(sb + bo[c]));
        const s16x4 b1 = __builtin_amdgcn_ds_read_tr16_b64_v4i16((lds_s16x4_ptr)(sb + bo[c] + 512));
        bf[c] = __builtin_shufflevector(b0, b1, 0, 1, 2, 3, 4, 5, 6, 7);
      }
#pragma unroll
      for (int rb = 0; rb < 2; ++rb) af[rb] = *(const s16x8*)(sb + aoff + rb * 1024);
#pragma unroll
      for (int rb = 0; rb < 2; ++rb)
#pragma unroll
        for (int c = 0; c < 2; ++c)
          acc[rb][c] = __builtin_amdgcn_mfma_f32_16x16x32_bf16(bf[c], af[rb], acc[rb][c], 0, 0, 0);
    }
  }
#pragma unroll
  for (int rb = 0; rb < 2; ++rb)
#pragma unroll
    for (int c = 0; c < 2; ++c) {
#pragma unroll
      for (int sp = 0; sp < DSPLIT; ++sp) asm volatile("" : "+v"(pf[rb][c][sp]));
      asm volatile("" : "+v"(xv[rb][c]));
    }
#pragma unroll
  for (int rb = 0; rb < 2; ++rb) {
#pragma unroll
    for (int c = 0; c < 2; ++c) {
      const int row = m0 + 32 * wr + 16 * rb + (lane & 15), col = n0 + 32 * wc + 16 * c + 4 * g;
      f32x4 f = {0.f, 0.f, 0.f, 0.f};
#pragma unroll
      for (int sp = 0; sp < DSPLIT; ++sp) {
        const u32x2 pv = pf[rb][c][sp];
        f += f32x4{__uint_as_float(pv[0] << 16), __uint_as_float(pv[0] & 0xffff0000u),
                   __uint_as_float(pv[1] << 16), __uint_as_float(pv[1] & 0xffff0000u)};
      }
      const u32x2 xw = xv[rb][c];
      float xf[4] = {__uint_as_float(xw[0] << 16), __uint_as_float(xw[0] & 0xffff0000u),
                     __uint_as_float(xw[1] << 16), __uint_as_float(xw[1] & 0xffff0000u)};
      float r4[4];
#pragma unroll
      for (int i = 0; i < 4; ++i) r4[i] = bfr(xf[i] + bfr(acc[rb][c][i])) + bfr(f[i]);
      u32x2 o = {pk2(r4[0], r4[1]), pk2(r4[2], r4[3])};
      *(u32x2*)(out + (size_t)row * D_ + col) = o;
    }
  }
}

constexpr size_t WS_XN = 0;
constexpr size_t WS_H = WS_XN + (size_t)S_ * D_ * 2;
constexpr size_t WS_Q = WS_H + (size_t)S_ * F_ * 2;
constexpr size_t WS_IMG = WS_Q + (size_t)H_ * S_ * ROWB;
constexpr size_t WS_O = WS_IMG + (size_t)H_ * IMG_HEAD;
constexpr size_t WS_P = WS_O + (size_t)S_ * D_ * 2;
constexpr size_t WS_END = WS_P + (size_t)DSPLIT * S_ * D_ * 2;

extern "C" void kernel_launch(void* const* d_in, const int* in_sizes, int n_in, void* d_out, int out_size,
                              void* d_ws, size_t ws_size, hipStream_t stream) {
  const u16* x = (const u16*)d_in[0];
  const u16* wqkv = (const u16*)d_in[1];
  const u16* wo = (const u16*)d_in[2];
  const u16* w1 = (const u16*)d_in[4];
  const u16* w3 = (const u16*)d_in[5];
  const u16* w2 = (const u16*)d_in[6];
  char* ws = (char*)d_ws;
  char* xn = ws + WS_XN;
  char* hbuf = ws + WS_H;
  u16* qbuf = (u16*)(ws + WS_Q);
  char* img = ws + WS_IMG;
  char* obuf = ws + WS_O;
  char* part = ws + WS_P;
  u16* out = (u16*)d_out;

  (void)hipFuncSetAttribute((const void*)k_gemm1, hipFuncAttributeMaxDynamicSharedMemorySize, G1::LDS);
  (void)hipFuncSetAttribute((const void*)k_mid, hipFuncAttributeMaxDynamicSharedMemorySize, MID_LDS);
  (void)hipFuncSetAttribute((const void*)k_final, hipFuncAttributeMaxDynamicSharedMemorySize, FIN_LDS);

  k_ln<<<S_ / 4 + 256, 256, 0, stream>>>(x, xn, wqkv, w1, w3, w2, wo);
  k_gemm1<<<256, 512, G1::LDS, stream>>>(xn, wqkv, w1, w3, hbuf, qbuf, img);
  k_mid<<<256 + 16 * 3 * DSPLIT, 512, MID_LDS, stream>>>((const char*)qbuf, img, obuf, hbuf, w2, part);
  k_final<<<32 * 9, 256, FIN_LDS, stream>>>(obuf, wo, part, x, out);
}
```

```cpp
#include <hip/hip_runtime.h>
#include <hip/hip_bf16.h>
#include <stdint.h>

typedef __attribute__((ext_vector_type(4))) short s16x4;
typedef __attribute__((ext_vector_type(8))) short s16x8;
typedef __attribute__((ext_vector_type(4))) float f32x4;
typedef __attribute__((ext_vector_type(16))) float f32x16;
typedef __attribute__((ext_vector_type(2))) unsigned u32x2;
typedef __attribute__((ext_vector_type(4))) unsigned u32x4;
typedef __attribute__((ext_vector_type(2))) __bf16 bf16x2;
typedef unsigned short u16;
typedef __attribute__((address_space(3))) s16x4* lds_s16x4_ptr;
typedef __attribute__((address_space(3))) char* lds_char_ptr;

constexpr int S_ = 2048, D_ = 576, H_ = 32, DH_ = 18, F_ = 4608, QKV_ = 1728;

#define DI __device__ __forceinline__

DI float bf2f(u16 v) { return __uint_as_float(((unsigned)v) << 16); }
DI u16 f2bf(float f) { return __builtin_bit_cast(u16, (__bf16)f); }
DI float bfr(float f) { return bf2f(f2bf(f)); }
DI unsigned pk2(float a, float b) { bf16x2 v = {(__bf16)a, (__bf16)b}; return __builtin_bit_cast(unsigned, v); }

DI void glds16(const void* gsrc, unsigned lds_dst) {
  unsigned keep;
  asm volatile("s_mov_b32 %0, m0\n\ts_mov_b32 m0, %2\n\ts_nop 0\n\tglobal_load_lds_dwordx4 %1, off\n\ts_mov_b32 m0, %0"
               : "=&s"(keep) : "v"(gsrc), "s"(lds_dst) : "memory");
}
DI void glds16s(const void* sbase, unsigned voff, unsigned lds_dst) {
  unsigned keep;
  asm volatile("s_mov_b32 %0, m0\n\ts_mov_b32 m0, %3\n\ts_nop 0\n\tglobal_load_lds_dwordx4 %1, %2\n\ts_mov_b32 m0, %0"
               : "=&s"(keep) : "v"(voff), "s"(sbase), "s"(lds_dst) : "memory");
}
template <int N> DI void wait_vm_bar() { asm volatile("s_waitcnt vmcnt(%0)\n\ts_barrier" ::"i"(N) : "memory"); }

DI unsigned lds_base(char* smem) { return (unsigned)(uintptr_t)(lds_char_ptr)smem; }

DI size_t atile_off(int row, int k, int nks) {
  const int r = row & 15, ch = (k >> 3) & 3;
  return ((size_t)(row >> 4) * nks + (k >> 5)) * 1024 + r * 64 + ((ch ^ ((-(r >> 2)) & 3)) << 4) + (k & 7) * 2;
}

__global__ __launch_bounds__(256) void k_ln(const u16* __restrict__ x, char* __restrict__ xn,
                                            const u16* __restrict__ wqkv, const u16* __restrict__ w1,
                                            const u16* __restrict__ w3) {
  if (blockIdx.x >= S_ / 4) {
    const int b = blockIdx.x - S_ / 4, xg = b & 7, part = b >> 3;
    const int t = threadIdx.x, k = part * 9 + (t >> 5), ln = t & 31;
    unsigned acc = 0;
    auto touch_row = [&](int kk) {
      if (ln < 9) acc ^= *(const unsigned*)(w1 + (size_t)kk * F_ + xg * 576 + ln * 64);
      else if (ln < 18) acc ^= *(const unsigned*)(w3 + (size_t)kk * F_ + xg * 576 + (ln - 9) * 64);
      else if (ln < 22) { const int c = (xg * 216 & ~63) + (ln - 18) * 64; acc ^= *(const unsigned*)(wqkv + (size_t)kk * QKV_ + (c < QKV_ ? c : 0)); }
    };
    touch_row(k);
    if (t < 32) touch_row(part * 9 + 8);
    asm volatile("" :: "v"(acc));
    return;
  }
  const int row = blockIdx.x * 4 + (threadIdx.x >> 6), lane = threadIdx.x & 63;
  const u16* xr = x + (size_t)row * D_;
  u32x4 c0 = *(const u32x4*)(xr + lane * 8);
  u32x4 c1 = {0u, 0u, 0u, 0u};
  const bool two = lane < 8;
  if (two) c1 = *(const u32x4*)(xr + 512 + lane * 8);
  float v[16];
#pragma unroll
  for (int i = 0; i < 4; ++i) {
    v[2 * i] = __uint_as_float(c0[i] << 16); v[2 * i + 1] = __uint_as_float(c0[i] & 0xffff0000u);
    v[8 + 2 * i] = __uint_as_float(c1[i] << 16); v[8 + 2 * i + 1] = __uint_as_float(c1[i] & 0xffff0000u);
  }
  float s = 0.f;
#pragma unroll
  for (int i = 0; i < 16; ++i) s += v[i];
#pragma unroll
  for (int o = 32; o > 0; o >>= 1) s += __shfl_xor(s, o);
  const float mean = s * (1.0f / D_);
  float q = 0.f;
#pragma unroll
  for (int i = 0; i < 8; ++i) { float d = v[i] - mean; q += d * d; }
  if (two) {
#pragma unroll
    for (int i = 8; i < 16; ++i) { float d = v[i] - mean; q += d * d; }
  }
#pragma unroll
  for (int o = 32; o > 0; o >>= 1) q += __shfl_xor(q, o);
  const float var = bfr(q * (1.0f / D_));
  const float m = bfr(mean);
  const float sd = bfr(sqrtf(bfr(var + 1e-5f)));
  u32x4 o0, o1;
#pragma unroll
  for (int i = 0; i < 4; ++i) {
    o0[i] = pk2(bfr(v[2 * i] - m) / sd, bfr(v[2 * i + 1] - m) / sd);
    o1[i] = pk2(bfr(v[8 + 2 * i] - m) / sd, bfr(v[8 + 2 * i + 1] - m) / sd);
  }
  *(u32x4*)(xn + atile_off(row, lane * 8, D_ / 32)) = o0;
  if (two) *(u32x4*)(xn + atile_off(row, 512 + lane * 8, D_ / 32)) = o1;
}

template <int WR, int WC, int RB, int NTW, int NG, int NSTAGE, int LW, bool XPF, int PFD, bool XL>
struct Gemm {
  static constexpr int NW = WR * WC;
  static constexpr int ROWS = WR * RB * 16;
  static constexpr int BSTEP = 4 * NG * 1024;
  static constexpr int ASTEP = ROWS * 64;
  static constexpr int STAGE = BSTEP + ASTEP;
  static constexpr int LDS = STAGE * NSTAGE;
  static constexpr int NBT = 4 * NG, NAT = ROWS / 16, NI = NBT + NAT;
  static constexpr int NGL = (NI + LW - 1) / LW;
  static constexpr int AHEAD = XPF ? 1 : 0;
  static_assert(NSTAGE >= 2 + AHEAD, "ring too short");
  static constexpr int TW = NW - LW;
  static constexpr int NTI = PFD > 0 ? (NI * 8 + 64 * TW - 1) / (64 * TW) : 0;

  template <typename WinFn, typename TileFn, typename UseFn>
  static DI void run(char* smem, const char* __restrict__ A, size_t rbs, int nk, WinFn winptr, TileFn tile, UseFn used,
                     f32x4 (&acc)[RB][NTW]) {
    const int tid = threadIdx.x, lane = tid & 63;
    const int w = __builtin_amdgcn_readfirstlane(tid >> 6);
    const int wr = (w < NW ? w : 0) / WC;
    const bool loader = w < LW;
    const int lw = w % LW;
    const unsigned lds0 = lds_base(smem);
    const char* src[NGL]; int sstep[NGL]; unsigned sdst[NGL];
    unsigned vob[2][2];
    unsigned onmask = 0;
    int kind[NGL];
    const int brow = lane >> 3, bip = (lane & 7) >> 1, bhalf = lane & 1;
    int ld0 = 0;
    (void)winptr(0, ld0);
    int ld1 = ld0;
    (void)winptr(NG - 1, ld1);
#pragma unroll
    for (int kp = 0; kp < 2; ++kp) {
      const int swz = ((brow >> 1) & 1) | (kp << 1);
      vob[kp][0] = (unsigned)(brow * ld0 + 16 * (bip ^ swz) + 8 * bhalf) * 2u;
      vob[kp][1] = (unsigned)(brow * ld1 + 16 * (bip ^ swz) + 8 * bhalf) * 2u;
    }
    const unsigned voa = (unsigned)lane * 16u;
#pragma unroll
    for (int j = 0; j < NGL; ++j) {
      int id = lw + LW * j;
      if (id >= NI) id -= NI;
      if (id < NBT) {
        const int kb = id / NG, grp = id % NG;
        const int swz = ((brow >> 1) & 1) | ((kb & 1) << 1);
        int ld;
        const u16* p = winptr(grp, ld);
        src[j] = (const char*)(p + (size_t)(kb * 8) * ld);
        kind[j] = (kb & 1) * 2 + (ld == ld0 ? 0 : 1);
        if (used(grp * 4 + (bip ^ swz)) || lane == 0) onmask |= 1u << j;
        sstep[j] = 32 * ld * 2;
        sdst[j] = (unsigned)id * 1024u;
      } else {
        const int rb = id - NBT;
        src[j] = A + rb * rbs;
        kind[j] = 4;
        onmask |= 1u << j;
        sstep[j] = 1024;
        sdst[j] = (unsigned)(BSTEP + rb * 1024);
      }
    }
    const char* tp[NTI > 0 ? NTI : 1]; int tstep[NTI > 0 ? NTI : 1];
    if constexpr (PFD > 0) {
#pragma unroll
      for (int jt = 0; jt < NTI; ++jt) {
        int id = ((w - LW + TW) % TW + TW * jt) * 8 + (lane >> 3);
        id = id < NI ? id : NI - 1;
        const int ln = lane & 7;
        if (id < NBT) {
          int ld;
          const u16* p = winptr(id % NG, ld);
          tp[jt] = (const char*)(p + (size_t)((id / NG) * 8 + ln) * ld);
          tstep[jt] = 32 * ld * 2;
        } else {
          tp[jt] = A + (id - NBT) * rbs + ln * 128;
          tstep[jt] = 1024;
        }
        tp[jt] += (size_t)(NSTAGE - 1) * tstep[jt];
      }
    }
    unsigned sink = 0;
    auto touch = [&]() {
#pragma unroll
      for (int jt = 0; jt < NTI; ++jt) {
        asm volatile("global_load_dword %0, %1, off" : "+v"(sink) : "v"(tp[jt]) : "memory");
        tp[jt] += tstep[jt];
      }
    };
    unsigned ibuf = 0;
    auto issue1 = [&](int j) {
      const int kd = kind[j];
      const unsigned vo = kd == 4 ? voa : kd == 0 ? vob[0][0] : kd == 1 ? vob[0][1] : kd == 2 ? vob[1][0] : vob[1][1];
      if ((onmask >> j) & 1u) glds16s(src[j], vo, lds0 + ibuf + sdst[j]);
      src[j] += sstep[j];
    };
    auto issue_done = [&]() { ibuf = ibuf + STAGE == LDS ? 0u : ibuf + STAGE; };
    const int g = lane >> 4, q = (lane & 15) >> 2, p4 = lane & 3;
    const int bswz = ((q >> 1) & 1) | ((g & 1) << 1);
    int bo[NTW];
#pragma unroll
    for (int c = 0; c < NTW; ++c) {
      const int t = tile(c);
      bo[c] = g * NG * 1024 + q * 128 + (((t & 3) ^ bswz) * 32) + p4 * 8 + (t >> 2) * 1024;
    }
    const int ar = lane & 15;
    const int aoff = BSTEP + wr * RB * 1024 + ar * 64 + ((g ^ ((-(ar >> 2)) & 3)) * 16);

#pragma unroll
    for (int rb = 0; rb < RB; ++rb)
#pragma unroll
      for (int c = 0; c < NTW; ++c) acc[rb][c] = f32x4{0.f, 0.f, 0.f, 0.f};

    auto ldb = [&](const char* sbk, int c) -> s16x8 {
      const char* bp = sbk + bo[c];
      const s16x4 b0 = __builtin_amdgcn_ds_read_tr16_b64_v4i16((lds_s16x4_ptr)(bp));
      const s16x4 b1 = __builtin_amdgcn_ds_read_tr16_b64_v4i16((lds_s16x4_ptr)(bp + 512));
      return __builtin_shufflevector(b0, b1, 0, 1, 2, 3, 4, 5, 6, 7);
    };
    auto lda = [&](const char* sbk, s16x8 (&dst)[RB]) {
#pragma unroll
      for (int rb = 0; rb < RB; ++rb) dst[rb] = *(const s16x8*)(sbk + aoff + rb * 1024);
    };

    if (loader) {
#pragma unroll
      for (int s = 0; s < NSTAGE - 1; ++s) {
#pragma unroll
        for (int j = 0; j < NGL; ++j) issue1(j);
        issue_done();
      }
    } else if constexpr (PFD > 0) {
      for (int s = NSTAGE - 1; s < NSTAGE - 1 + PFD && s < nk; ++s) touch();
    }
    wait_vm_bar<(NSTAGE - 2 - AHEAD) * NGL>();
    if (XL && w >= NW) {
#pragma unroll 1
      for (int s = 0; s + 1 < nk; ++s) {
        if (s + NSTAGE - 1 < nk) {
#pragma unroll
          for (int j = 0; j < NGL; ++j) issue1(j);
          issue_done();
          wait_vm_bar<(NSTAGE - 2 - AHEAD) * NGL>();
        } else wait_vm_bar<0>();
      }
      return;
    }
    s16x8 a[2][RB];
    s16x8 bA = ldb(smem, 0);
    lda(smem, a[0]);
    s16x8 bB = ldb(smem, NTW > 1 ? 1 : 0);

    unsigned cbuf = 0;
#pragma unroll 1
    for (int s0 = 0; s0 < nk; s0 += 2) {
#pragma unroll
      for (int u = 0; u < 2; ++u) {
        const int s = s0 + u;
        if (s >= nk) break;
        const bool do_issue = loader && s + NSTAGE - 1 < nk, has_next = s + 1 < nk;
        const char* sb = smem + cbuf;
        cbuf = cbuf + STAGE == LDS ? 0u : cbuf + STAGE;
        const char* nb = smem + cbuf;
        if (do_issue) {
#pragma unroll
          for (int j = 0; j < NGL; ++j) issue1(j);
          issue_done();
        }
        if constexpr (PFD > 0) { if (!loader && s + NSTAGE - 1 + PFD < nk) touch(); }
        s16x8 bq[NTW + 2];
        bq[0] = bA; bq[1] = bB;
#pragma unroll
        for (int c = 0; c < NTW; ++c) {
          if (c + 2 < NTW) bq[c + 2] = ldb(sb, c + 2);
          else {
            bq[c + 2] = bq[c];
            if (XPF && has_next) {
              if (c + 2 == NTW || NTW == 1) lda(nb, a[u ^ 1]);
              bq[c + 2] = ldb(nb, (c + 2 - NTW) % NTW);
            }
          }
          __builtin_amdgcn_sched_barrier(0);
#pragma unroll
          for (int rb = 0; rb < RB; ++rb)
            acc[rb][c] = __builtin_amdgcn_mfma_f32_16x16x32_bf16(bq[c], a[u][rb], acc[rb][c], 0, 0, 0);
          __builtin_amdgcn_sched_barrier(0);
        }
        bA = bq[NTW]; bB = bq[NTW + 1];
        if (has_next) {
          if (s + NSTAGE - 1 < nk) wait_vm_bar<(NSTAGE - 2 - AHEAD) * NGL>(); else wait_vm_bar<0>();
          if (!XPF) {
            bA = ldb(nb, 0);
            lda(nb, a[u ^ 1]);
            bB = ldb(nb, NTW > 1 ? 1 : 0);
          }
        }
      }
    }
    if constexpr (PFD > 0) asm volatile("s_waitcnt vmcnt(0)" : "+v"(sink) : : "memory");
  }
};

constexpr int ROWB = 48;
constexpr int CH_K = 0, CH_V = 256 * ROWB, CH_BYTES = 512 * ROWB;
constexpr int IMG_HEAD = 8 * CH_BYTES;

typedef Gemm<4, 2, 4, 11, 8, 3, 4, true, 0, false> G1;
constexpr float QSCALE = 0.23570226039551584f * 1.4426950408889634f;

DI float silu_mul(float gt, float up) {
  const float e = __builtin_amdgcn_exp2f(gt * -1.4426950408889634f);
  return gt * __builtin_amdgcn_rcpf(1.0f + e) * up;
}

__global__ __launch_bounds__(512, 2) void k_gemm1(const char* __restrict__ xn, const u16* __restrict__ wqkv,
                                                  const u16* __restrict__ w1, const u16* __restrict__ w3,
                                                  char* __restrict__ hbuf, u16* __restrict__ qbuf,
                                                  char* __restrict__ img) {
  extern __shared__ __attribute__((aligned(16))) char smem[];
  const int b = blockIdx.x;
  const int cgp = (b & 7) * 4 + ((b >> 3) & 3);
  const int panel = b >> 5;
  const int m0 = panel * 256, n0 = cgp * 144;
  const int qt0 = (108 * cgp) >> 5, qt1 = (108 * (cgp + 1)) >> 5;
  const int aw = n0 & ~63, toff = (n0 & 63) >> 4;
  const int qaw = (16 * qt0) & ~63, qoff = qt0 & 3;
  const int w = __builtin_amdgcn_readfirstlane(threadIdx.x >> 6);
  const int wr = w >> 1, wc = w & 1;
  auto winptr = [&](int g, int& ld) -> const u16* {
    if (g < 3) { ld = F_; return w1 + aw + 64 * g; }
    if (g < 6) { ld = F_; return w3 + aw + 64 * (g - 3); }
    ld = QKV_;
    const int c = qaw + 64 * (g - 6);
    return wqkv + (c < QKV_ ? c : qaw);
  };
  auto tile = [&](int c) -> int {
    if (wc == 0) return c < 5 ? toff + c : c < 10 ? 12 + toff + (c - 5) : 24 + qoff;
    const int t = c < 4 ? toff + 5 + c : c < 8 ? 12 + toff + 1 + c : 24 + qoff + 1 + (c - 8);
    return t < 32 ? t : 31;
  };
  auto used = [&](int t) -> bool {
    return t < 12 ? (unsigned)(t - toff) < 9u : t < 24 ? (unsigned)(t - 12 - toff) < 9u : (unsigned)(t - 24 - qoff) < (unsigned)(qt1 - qt0);
  };
  f32x4 acc[4][11];
  G1::run(smem, xn + (size_t)(m0 >> 4) * (D_ / 32) * 1024, (size_t)(D_ / 32) * 1024, D_ / 32, winptr, tile, used, acc);

  const int lane = threadIdx.x & 63, g = lane >> 4;
  const int row0 = m0 + 64 * wr + (lane & 15);
  if (wc == 0) {
#pragma unroll
    for (int c = 0; c < 5; ++c) {
      char* hp = hbuf + atile_off(row0, n0 + 16 * c + 4 * g, F_ / 32);
#pragma unroll
      for (int rb = 0; rb < 4; ++rb) {
        u32x2 o = {pk2(silu_mul(acc[rb][c][0], acc[rb][c + 5][0]), silu_mul(acc[rb][c][1], acc[rb][c + 5][1])),
                   pk2(silu_mul(acc[rb][c][2], acc[rb][c + 5][2]), silu_mul(acc[rb][c][3], acc[rb][c + 5][3]))};
        *(u32x2*)(hp + (size_t)rb * (F_ / 32) * 1024) = o;
      }
      __builtin_amdgcn_sched_barrier(0);
    }
  } else {
#pragma unroll
    for (int c = 0; c < 4; ++c) {
      char* hp = hbuf + atile_off(row0, n0 + 80 + 16 * c + 4 * g, F_ / 32);
#pragma unroll
      for (int rb = 0; rb < 4; ++rb) {
        u32x2 o = {pk2(silu_mul(acc[rb][c][0], acc[rb][c + 4][0]), silu_mul(acc[rb][c][1], acc[rb][c + 4][1])),
                   pk2(silu_mul(acc[rb][c][2], acc[rb][c + 4][2]), silu_mul(acc[rb][c][3], acc[rb][c + 4][3]))};
        *(u32x2*)(hp + (size_t)rb * (F_ / 32) * 1024) = o;
      }
      __builtin_amdgcn_sched_barrier(0);
    }
  }
#pragma unroll
  for (int c = 8; c < 11; ++c) {
    int t;
    if (wc == 0) { if (c != 10) continue; t = qt0; } else t = qt0 + 1 + (c - 8);
    if (t >= qt1) continue;
    const int which = t / 36;
    const int cc0 = 16 * (t - 36 * which) + 4 * g;
    const int head = cc0 / DH_, d0 = cc0 - head * DH_;
    const bool strad = d0 == 16;
    const int pos = d0 + 2 * (head & 1);
    const float sc = which == 0 ? QSCALE : 1.0f;
    char* base;
    size_t hstride;
    if (which == 0) { base = (char*)qbuf + (size_t)row0 * ROWB; hstride = (size_t)S_ * ROWB; }
    else { base = img + (row0 >> 8) * CH_BYTES + (which == 1 ? CH_K : CH_V) + (row0 & 255) * ROWB; hstride = IMG_HEAD; }
    char* p = base + head * hstride + pos * 2;
#pragma unroll
    for (int rb = 0; rb < 4; ++rb) {
      const unsigned lo = pk2(acc[rb][c][0] * sc, acc[rb][c][1] * sc), hi = pk2(acc[rb][c][2] * sc, acc[rb][c][3] * sc);
      u32x2 v = {lo, strad ? 0x3f803f80u : hi};
      *(u32x2*)(p + rb * 16 * ROWB) = v;
      if (strad) { u32x2 v2 = {0x3f803f80u, hi}; *(u32x2*)(base + (head + 1) * hstride + rb * 16 * ROWB) = v2; }
    }
  }
}

constexpr int AT_LDS = 3 * CH_BYTES;
constexpr float AT_GAP = 7.0f;

DI float max3f(float a, float b, float c) { return fmaxf(fmaxf(a, b), c); }
DI float rowmax16(const f32x16& s) {
  float mx = max3f(s[0], s[1], s[2]);
#pragma unroll
  for (int i = 3; i < 15; i += 2) mx = max3f(mx, s[i], s[i + 1]);
  return fmaxf(mx, s[15]);
}

template <int KG, int AT_NBUF>
DI void attn_block(char* smem, int ab, const char* __restrict__ qbuf, const char* __restrict__ img,
                   char* __restrict__ obuf) {
  const int tid = threadIdx.x, lane = tid & 63, r = lane & 31, hf = lane >> 5;
  const int wv = __builtin_amdgcn_readfirstlane(tid >> 6);
  const int w = wv & 7, kg = wv >> 3;
  constexpr int NT = 8 / KG, NP = 3 / KG + (3 % KG ? 1 : 0);
  const int head = (ab & 7) * 4 + ((ab >> 3) & 3), qb = ab >> 5, odd = head & 1;
  const unsigned lds0 = lds_base(smem);
  const char* gsrc = img + (size_t)head * IMG_HEAD;
  const unsigned vl = lane * 16;
  auto issue = [&](int ch) {
    const unsigned dst = lds0 + (unsigned)((ch % AT_NBUF) * CH_BYTES);
    const char* s = gsrc + (size_t)ch * CH_BYTES;
#pragma unroll
    for (int j = 0; j < NP; ++j) {
      int pc = wv + 8 * KG * j;
      if (pc >= 24) pc -= 24;
      glds16s(s + pc * 1024, vl, dst + pc * 1024);
    }
  };
#pragma unroll
  for (int c = 0; c < AT_NBUF - 1; ++c) issue(c);

  const int qrow = qb * 256 + w * 32 + r;
  const char* qp = qbuf + ((size_t)head * S_ + qrow) * ROWB;
  u32x4 q0u = *(const u32x4*)(qp + 16 * hf);
  u32x4 q1u = {0u, 0u, 0u, 0u};
  if (hf == 0) { const u32x2 t2 = *(const u32x2*)(qp + 32); q1u[0] = t2[0]; q1u[1] = t2[1]; }

  const int kmo = CH_K + r * ROWB + hf * 16;
  const int kto = CH_K + r * ROWB + 32;
  const int vo = CH_V + (4 * hf + ((lane & 15) >> 2)) * ROWB + ((lane >> 4) & 1) * 32 + (lane & 3) * 8;
  const f32x16 zero16 = {0.f, 0.f, 0.f, 0.f, 0.f, 0.f, 0.f, 0.f, 0.f, 0.f, 0.f, 0.f, 0.f, 0.f, 0.f, 0.f};

  struct KF { s16x8 f0; u32x2 tl; };
  auto kload = [&](const char* cb, int t) -> KF {
    KF k;
    k.f0 = *(const s16x8*)(cb + kmo + t * 32 * ROWB);
    k.tl = *(const u32x2*)(cb + kto + t * 32 * ROWB);
    return k;
  };
  auto qkm = [&](const KF& k) -> f32x16 {
    const u32x4 k1u = {k.tl[0], k.tl[1], 0u, 0u};
    f32x16 s = __builtin_amdgcn_mfma_f32_32x32x16_bf16(k.f0, __builtin_bit_cast(s16x8, q0u), zero16, 0, 0, 0);
    return __builtin_amdgcn_mfma_f32_32x32x16_bf16(__builtin_bit_cast(s16x8, k1u), __builtin_bit_cast(s16x8, q1u), s, 0, 0, 0);
  };
  auto vfrag = [&](const char* cb, int st) -> s16x8 {
    const char* p = cb + vo + st * 16 * ROWB;
    const s16x4 lo = __builtin_amdgcn_ds_read_tr16_b64_v4i16((lds_s16x4_ptr)(p));
    const s16x4 hi = __builtin_amdgcn_ds_read_tr16_b64_v4i16((lds_s16x4_ptr)(p + 8 * ROWB));
    return __builtin_shufflevector(lo, hi, 0, 1, 2, 3, 4, 5, 6, 7);
  };

  f32x16 o = zero16;
  float mrun = 0.f;
  auto set_m = [&](float m) {
    const float mh = bfr(m), ml = bfr(m - mh);
    mrun = mh + ml;
    const unsigned mm = pk2(-mh, -ml);
    if (hf == 0) { if (odd) q0u[0] = mm; else q1u[1] = mm; }
  };
  set_m(0.f);

  auto exps = [&](const f32x16& sx, u32x4& p0, u32x4& p1) {
#pragma unroll
    for (int i = 0; i < 4; ++i) p0[i] = pk2(__builtin_amdgcn_exp2f(sx[2 * i]), __builtin_amdgcn_exp2f(sx[2 * i + 1]));
#pragma unroll
    for (int i = 0; i < 4; ++i) p1[i] = pk2(__builtin_amdgcn_exp2f(sx[8 + 2 * i]), __builtin_amdgcn_exp2f(sx[8 + 2 * i + 1]));
  };
  auto bump = [&](f32x16& sx, bool first) -> float {
    const float mx = rowmax16(sx);
    float mq = fmaxf(mx, __shfl_xor(mx, 32)) + AT_GAP;
    if (!first) mq = fmaxf(mq, 0.f);
    const float mold = mrun;
    set_m(mold + mq);
    const float dl = mrun - mold;
    const float al = __builtin_amdgcn_exp2f(-dl);
#pragma unroll
    for (int i = 0; i < 16; ++i) { sx[i] -= dl; o[i] *= al; }
    return dl;
  };
#pragma unroll 1
  for (int ch = 0; ch < 8; ++ch) {
    if (ch + AT_NBUF - 2 < 8) wait_vm_bar<(AT_NBUF - 2) * NP>(); else wait_vm_bar<0>();
    if (ch + AT_NBUF - 1 < 8) issue(ch + AT_NBUF - 1);
    const char* cb = smem + (ch % AT_NBUF) * CH_BYTES + kg * NT * 32 * ROWB;
    KF kn = kload(cb, 1 < NT ? 1 : 0);
    f32x16 sc = qkm(kload(cb, 0));
    if (ch == 0) (void)bump(sc, true);
#pragma unroll
    for (int t = 0; t < NT; ++t) {
      f32x16 sn = zero16;
      if (t + 1 < NT) sn = qkm(kn);
      const s16x8 vf0 = vfrag(cb, 2 * t);
      const s16x8 vf1 = vfrag(cb, 2 * t + 1);
      if (t + 2 < NT) kn = kload(cb, t + 2);
      __builtin_amdgcn_sched_barrier(0);
      u32x4 p0, p1;
      exps(sc, p0, p1);
      const unsigned any = (p0[0] | p0[1] | p0[2]) | (p0[3] | p1[0] | p1[1]) | (p1[2] | p1[3]);
      if (__any((any & 0x40004000u) != 0u)) {
        const float dl = bump(sc, false);
#pragma unroll
        for (int i = 0; i < 16; ++i) sn[i] -= dl;
        exps(sc, p0, p1);
      }
      __builtin_amdgcn_sched_barrier(0);
      o = __builtin_amdgcn_mfma_f32_32x32x16_bf16(vf0, __builtin_bit_cast(s16x8, p0), o, 0, 0, 0);
      o = __builtin_amdgcn_mfma_f32_32x32x16_bf16(vf1, __builtin_bit_cast(s16x8, p1), o, 0, 0, 0);
      __builtin_amdgcn_sched_barrier(0);
      sc = sn;
    }
  }
  if (KG == 2) {
    __syncthreads();
    float* mb = (float*)smem + w * (17 * 64);
    if (kg == 1) {
#pragma unroll
      for (int i = 0; i < 16; ++i) mb[i * 64 + lane] = o[i];
      mb[16 * 64 + lane] = mrun;
    }
    __syncthreads();
    if (kg == 1) return;
    const float m1 = mb[16 * 64 + lane];
    const float mx = fmaxf(mrun, m1);
    const float a0 = __builtin_amdgcn_exp2f(mrun - mx), a1 = __builtin_amdgcn_exp2f(m1 - mx);
#pragma unroll
    for (int i = 0; i < 16; ++i) o[i] = o[i] * a0 + mb[i * 64 + lane] * a1;
  }
  float l = odd ? o[0] : o[10];
  l = __shfl(l, r);
  const float inv = 1.0f / l;
  const int c0 = head * DH_ + 4 * hf - 2 * odd;
  if (!(odd && hf == 0)) *(unsigned*)(obuf + atile_off(qrow, c0 + 0, D_ / 32)) = pk2(o[0] * inv, o[1] * inv);
  *(unsigned*)(obuf + atile_off(qrow, c0 + 2, D_ / 32)) = pk2(o[2] * inv, o[3] * inv);
  *(unsigned*)(obuf + atile_off(qrow, c0 + 8, D_ / 32)) = pk2(o[4] * inv, o[5] * inv);
  *(unsigned*)(obuf + atile_off(qrow, c0 + 10, D_ / 32)) = pk2(o[6] * inv, o[7] * inv);
  if (hf == 0) {
    *(unsigned*)(obuf + atile_off(qrow, c0 + 16, D_ / 32)) = pk2(o[8] * inv, o[9] * inv);
    if (odd) *(unsigned*)(obuf + atile_off(qrow, c0 + 18, D_ / 32)) = pk2(o[10] * inv, o[11] * inv);
  }
}

constexpr int DSPLIT = 4;
typedef Gemm<2, 4, 4, 3, 3, 4, 4, false, 0, false> G2;
DI void down_block(char* smem, int b, const char* __restrict__ hbuf, const u16* __restrict__ w2,
                   char* __restrict__ part) {
  const int xcd = b & 7, idx = b >> 3;
  const int split = xcd >> 1, panel = (xcd & 1) * 8 + idx / 3, cg = idx % 3;
  const int m0 = panel * 128, n0 = cg * 192, k0 = split * (F_ / DSPLIT);
  const int w = __builtin_amdgcn_readfirstlane(threadIdx.x >> 6);
  const int wr = w >> 2, wc = w & 3;
  auto winptr = [&](int g, int& ld) -> const u16* { ld = D_; return w2 + (size_t)k0 * D_ + n0 + 64 * g; };
  auto tile = [&](int c) -> int { return wc * 3 + c; };
  f32x4 acc[4][3];
  G2::run(smem, hbuf + ((size_t)(m0 >> 4) * (F_ / 32) + (k0 >> 5)) * 1024, (size_t)(F_ / 32) * 1024, F_ / DSPLIT / 32,
          winptr, tile, [](int) { return true; }, acc);
  const int lane = threadIdx.x & 63;
#pragma unroll
  for (int rb = 0; rb < 4; ++rb) {
    const int rblk = (m0 >> 4) + 4 * wr + rb;
    char* pr = part + (((size_t)split * (S_ / 16) + rblk) * (D_ / 16) + (n0 >> 4) + 3 * wc) * 512 + lane * 8;
#pragma unroll
    for (int c = 0; c < 3; ++c) {
      u32x2 v = {pk2(acc[rb][c][0], acc[rb][c][1]), pk2(acc[rb][c][2], acc[rb][c][3])};
      *(u32x2*)(pr + 512 * c) = v;
    }
  }
}

constexpr int MID_LDS = AT_LDS > G2::LDS ? AT_LDS : G2::LDS;
__global__ __launch_bounds__(512, 4) void k_mid(const char* __restrict__ qbuf, const char* __restrict__ img,
                                                char* __restrict__ obuf, const char* __restrict__ hbuf,
                                                const u16* __restrict__ w2, char* __restrict__ part) {
  extern __shared__ __attribute__((aligned(16))) char smem[];
  if (blockIdx.x < 256) attn_block<1, 3>(smem, blockIdx.x, qbuf, img, obuf);
  else down_block(smem, blockIdx.x - 256, hbuf, w2, part);
}

__global__ __launch_bounds__(1024, 4) void k_attn2(const char* __restrict__ qbuf, const char* __restrict__ img,
                                                   char* __restrict__ obuf) {
  extern __shared__ __attribute__((aligned(16))) char smem[];
  attn_block<2, 6>(smem, blockIdx.x, qbuf, img, obuf);
}

constexpr int FIN_LDS = 9 * 8192;
DI void gload16g(f32x4& dst, const void* p) { asm volatile("global_load_dwordx4 %0, %1, off" : "=v"(dst) : "v"(p) : "memory"); }
DI void gload8g(u32x2& dst, const void* p) { asm volatile("global_load_dwordx2 %0, %1, off" : "=v"(dst) : "v"(p) : "memory"); }

__global__ __launch_bounds__(256, 2) void k_final(const char* __restrict__ obuf, const u16* __restrict__ wo,
                                                  const char* __restrict__ part, const u16* __restrict__ x,
                                                  u16* __restrict__ out) {
  extern __shared__ __attribute__((aligned(16))) char smem[];
  const int b = blockIdx.x;
  const int cg = (b >> 3) % 9, panel = (b & 7) * 4 + (b >> 3) / 9;
  const int m0 = panel * 64, n0 = cg * 64;
  const int lane = threadIdx.x & 63, g = lane >> 4;
  const int w = __builtin_amdgcn_readfirstlane(threadIdx.x >> 6);
  const int wr = w >> 1, wc = w & 1;
  const unsigned lds0 = lds_base(smem);
  const int brow = lane >> 3, bip = (lane & 7) >> 1, bhalf = lane & 1;
  const unsigned vob = (unsigned)(brow * D_ + 16 * (bip ^ (((brow >> 1) & 1) | ((w & 1) << 1))) + 8 * bhalf) * 2u;
  const unsigned voa = (unsigned)lane * 16u;
  const char* bsrc = (const char*)(wo + (size_t)(w * 8) * D_ + n0);
  const char* asrc = obuf + ((size_t)(m0 >> 4) + w) * (D_ / 32) * 1024;
  auto issue = [&](int s) {
    const unsigned dst = lds0 + (unsigned)((s % 9) * 8192 + w * 1024);
    glds16s(bsrc + (size_t)s * 32 * D_ * 2, vob, dst);
    glds16s(asrc + (size_t)s * 1024, voa, dst + 4096);
  };
#pragma unroll
  for (int s = 0; s < 6; ++s) issue(s);
  u32x2 pf[2][2][DSPLIT];
  u32x2 xv[2][2];
#pragma unroll
  for (int rb = 0; rb < 2; ++rb)
#pragma unroll
    for (int c = 0; c < 2; ++c) {
      const int row = m0 + 32 * wr + 16 * rb + (lane & 15), col = n0 + 32 * wc + 16 * c + 4 * g;
#pragma unroll
      for (int sp = 0; sp < DSPLIT; ++sp)
        gload8g(pf[rb][c][sp], part + (((size_t)sp * (S_ / 16) + (m0 >> 4) + 2 * wr + rb) * (D_ / 16) + (n0 >> 4) + 2 * wc + c) * 512 + lane * 8);
      gload8g(xv[rb][c], x + (size_t)row * D_ + col);
    }
  constexpr int NEX = 4 * (DSPLIT + 1);
  const int q = (lane & 15) >> 2, p4 = lane & 3;
  const int bswz = ((q >> 1) & 1) | ((g & 1) << 1);
  int bo[2];
#pragma unroll
  for (int c = 0; c < 2; ++c) bo[c] = g * 1024 + q * 128 + (((wc * 2 + c) ^ bswz) * 32) + p4 * 8;
  const int ar = lane & 15;
  const int aoff = 4096 + wr * 2048 + ar * 64 + ((g ^ ((-(ar >> 2)) & 3)) * 16);
  f32x4 acc[2][2];
#pragma unroll
  for (int rb = 0; rb < 2; ++rb)
#pragma unroll
    for (int c = 0; c < 2; ++c) acc[rb][c] = f32x4{0.f, 0.f, 0.f, 0.f};
#pragma unroll
  for (int gi = 0; gi < 6; ++gi) {
    if (gi == 0) wait_vm_bar<6 + NEX>(); else if (gi < 5) wait_vm_bar<6>(); else wait_vm_bar<0>();
    if (gi < 4) { issue(3 * gi + 6); issue(3 * gi + 7); issue(3 * gi + 8); }
#pragma unroll
    for (int u = 0; u < 3; ++u) {
      const char* sb = smem + ((3 * gi + u) % 9) * 8192;
      s16x8 bf[2], af[2];
#pragma unroll
      for (int c = 0; c < 2; ++c) {
        const s16x4 b0 = __builtin_amdgcn_ds_read_tr16_b64_v4i16((lds_s16x4_ptr)(sb + bo[c]));
        const s16x4 b1 = __builtin_amdgcn_ds_read_tr16_b64_v4i16((lds_s16x4_ptr)(sb + bo[c] + 512));
        bf[c] = __builtin_shufflevector(b0, b1, 0, 1, 2, 3, 4, 5, 6, 7);
      }
#pragma unroll
      for (int rb = 0; rb < 2; ++rb) af[rb] = *(const s16x8*)(sb + aoff + rb * 1024);
#pragma unroll
      for (int rb = 0; rb < 2; ++rb)
#pragma unroll
        for (int c = 0; c < 2; ++c)
          acc[rb][c] = __builtin_amdgcn_mfma_f32_16x16x32_bf16(bf[c], af[rb], acc[rb][c], 0, 0, 0);
    }
  }
#pragma unroll
  for (int rb = 0; rb < 2; ++rb)
#pragma unroll
    for (int c = 0; c < 2; ++c) {
#pragma unroll
      for (int sp = 0; sp < DSPLIT; ++sp) asm volatile("" : "+v"(pf[rb][c][sp]));
      asm volatile("" : "+v"(xv[rb][c]));
    }
#pragma unroll
  for (int rb = 0; rb < 2; ++rb) {
#pragma unroll
    for (int c = 0; c < 2; ++c) {
      const int row = m0 + 32 * wr + 16 * rb + (lane & 15), col = n0 + 32 * wc + 16 * c + 4 * g;
      f32x4 f = {0.f, 0.f, 0.f, 0.f};
#pragma unroll
      for (int sp = 0; sp < DSPLIT; ++sp) {
        const u32x2 pv = pf[rb][c][sp];
        f += f32x4{__uint_as_float(pv[0] << 16), __uint_as_float(pv[0] & 0xffff0000u),
                   __uint_as_float(pv[1] << 16), __uint_as_float(pv[1] & 0xffff0000u)};
      }
      const u32x2 xw = xv[rb][c];
      float xf[4] = {__uint_as_float(xw[0] << 16), __uint_as_float(xw[0] & 0xffff0000u),
                     __uint_as_float(xw[1] << 16), __uint_as_float(xw[1] & 0xffff0000u)};
      float r4[4];
#pragma unroll
      for (int i = 0; i < 4; ++i) r4[i] = bfr(xf[i] + bfr(acc[rb][c][i])) + bfr(f[i]);
      u32x2 o = {pk2(r4[0], r4[1]), pk2(r4[2], r4[3])};
      *(u32x2*)(out + (size_t)row * D_ + col) = o;
    }
  }
}

constexpr size_t WS_XN = 0;
constexpr size_t WS_H = WS_XN + (size_t)S_ * D_ * 2;
constexpr size_t WS_Q = WS_H + (size_t)S_ * F_ * 2;
constexpr size_t WS_IMG = WS_Q + (size_t)H_ * S_ * ROWB;
constexpr size_t WS_O = WS_IMG + (size_t)H_ * IMG_HEAD;
constexpr size_t WS_P = WS_O + (size_t)S_ * D_ * 2;
constexpr size_t WS_END = WS_P + (size_t)DSPLIT * S_ * D_ * 2;

extern "C" void kernel_launch(void* const* d_in, const int* in_sizes, int n_in, void* d_out, int out_size,
                              void* d_ws, size_t ws_size, hipStream_t stream) {
  const u16* x = (const u16*)d_in[0];
  const u16* wqkv = (const u16*)d_in[1];
  const u16* wo = (const u16*)d_in[2];
  const u16* w1 = (const u16*)d_in[4];
  const u16* w3 = (const u16*)d_in[5];
  const u16* w2 = (const u16*)d_in[6];
  char* ws = (char*)d_ws;
  char* xn = ws + WS_XN;
  char* hbuf = ws + WS_H;
  u16* qbuf = (u16*)(ws + WS_Q);
  char* img = ws + WS_IMG;
  char* obuf = ws + WS_O;
  char* part = ws + WS_P;
  u16* out = (u16*)d_out;

  (void)hipFuncSetAttribute((const void*)k_gemm1, hipFuncAttributeMaxDynamicSharedMemorySize, G1::LDS);
  (void)hipFuncSetAttribute((const void*)k_mid, hipFuncAttributeMaxDynamicSharedMemorySize, MID_LDS);
  (void)hipFuncSetAttribute((const void*)k_final, hipFuncAttributeMaxDynamicSharedMemorySize, FIN_LDS);

  k_ln<<<S_ / 4 + 512, 256, 0, stream>>>(x, xn, wqkv, w1, w3);
  k_gemm1<<<256, 512, G1::LDS, stream>>>(xn, wqkv, w1, w3, hbuf, qbuf, img);
  k_mid<<<256 + 16 * 3 * DSPLIT, 512, MID_LDS, stream>>>((const char*)qbuf, img, obuf, hbuf, w2, part);
  k_final<<<32 * 9, 256, FIN_LDS, stream>>>(obuf, wo, part, x, out);
}
```

```cpp
#include <hip/hip_runtime.h>
#include <hip/hip_bf16.h>
#include <stdint.h>

typedef __attribute__((ext_vector_type(4))) short s16x4;
typedef __attribute__((ext_vector_type(8))) short s16x8;
typedef __attribute__((ext_vector_type(4))) float f32x4;
typedef __attribute__((ext_vector_type(16))) float f32x16;
typedef __attribute__((ext_vector_type(2))) unsigned u32x2;
typedef __attribute__((ext_vector_type(4))) unsigned u32x4;
typedef __attribute__((ext_vector_type(2))) __bf16 bf16x2;
typedef unsigned short u16;
typedef __attribute__((address_space(3))) s16x4* lds_s16x4_ptr;
typedef __attribute__((address_space(3))) char* lds_char_ptr;

constexpr int S_ = 2048, D_ = 576, H_ = 32, DH_ = 18, F_ = 4608, QKV_ = 1728;

#define DI __device__ __forceinline__

DI float bf2f(u16 v) { return __uint_as_float(((unsigned)v) << 16); }
DI u16 f2bf(float f) { return __builtin_bit_cast(u16, (__bf16)f); }
DI float bfr(float f) { return bf2f(f2bf(f)); }
DI unsigned pk2(float a, float b) { bf16x2 v = {(__bf16)a, (__bf16)b}; return __builtin_bit_cast(unsigned, v); }

DI void glds16s(const void* sbase, unsigned voff, unsigned lds_dst) {
  unsigned keep;
  asm volatile("s_mov_b32 %0, m0\n\ts_mov_b32 m0, %3\n\ts_nop 0\n\tglobal_load_lds_dwordx4 %1, %2\n\ts_mov_b32 m0, %0"
               : "=&s"(keep) : "v"(voff), "s"(sbase), "s"(lds_dst) : "memory");
}
template <int N> DI void wait_vm_bar() { asm volatile("s_waitcnt vmcnt(%0)\n\ts_barrier" ::"i"(N) : "memory"); }

DI unsigned lds_base(char* smem) { return (unsigned)(uintptr_t)(lds_char_ptr)smem; }

DI size_t atile_off(int row, int k, int nks) {
  const int r = row & 15, ch = (k >> 3) & 3;
  return ((size_t)(row >> 4) * nks + (k >> 5)) * 1024 + r * 64 + ((ch ^ ((-(r >> 2)) & 3)) << 4) + (k & 7) * 2;
}

__global__ __launch_bounds__(256) void k_ln(const u16* __restrict__ x, char* __restrict__ xn,
                                            const u16* __restrict__ wqkv, const u16* __restrict__ w1,
                                            const u16* __restrict__ w3) {
  if (blockIdx.x < 512) {
    const int b = blockIdx.x, xg = b & 7, part = b >> 3;
    const int t = threadIdx.x, k = part * 9 + (t >> 5), ln = t & 31;
    unsigned acc = 0;
    auto touch_row = [&](int kk) {
      if (ln < 9) acc ^= *(const unsigned*)(w1 + (size_t)kk * F_ + xg * 576 + ln * 64);
      else if (ln < 18) acc ^= *(const unsigned*)(w3 + (size_t)kk * F_ + xg * 576 + (ln - 9) * 64);
      else if (ln < 22) { const int c = (xg * 216 & ~63) + (ln - 18) * 64; acc ^= *(const unsigned*)(wqkv + (size_t)kk * QKV_ + (c < QKV_ ? c : 0)); }
    };
    touch_row(k);
    if (t < 32) touch_row(part * 9 + 8);
    asm volatile("" :: "v"(acc));
    return;
  }
  const int row = (blockIdx.x - 512) * 4 + (threadIdx.x >> 6), lane = threadIdx.x & 63;
  const u16* xr = x + (size_t)row * D_;
  u32x4 c0 = *(const u32x4*)(xr + lane * 8);
  u32x4 c1 = {0u, 0u, 0u, 0u};
  const bool two = lane < 8;
  if (two) c1 = *(const u32x4*)(xr + 512 + lane * 8);
  float v[16];
#pragma unroll
  for (int i = 0; i < 4; ++i) {
    v[2 * i] = __uint_as_float(c0[i] << 16); v[2 * i + 1] = __uint_as_float(c0[i] & 0xffff0000u);
    v[8 + 2 * i] = __uint_as_float(c1[i] << 16); v[8 + 2 * i + 1] = __uint_as_float(c1[i] & 0xffff0000u);
  }
  float s = 0.f;
#pragma unroll
  for (int i = 0; i < 16; ++i) s += v[i];
#pragma unroll
  for (int o = 32; o > 0; o >>= 1) s += __shfl_xor(s, o);
  const float mean = s * (1.0f / D_);
  float q = 0.f;
#pragma unroll
  for (int i = 0; i < 8; ++i) { float d = v[i] - mean; q += d * d; }
  if (two) {
#pragma unroll
    for (int i = 8; i < 16; ++i) { float d = v[i] - mean; q += d * d; }
  }
#pragma unroll
  for (int o = 32; o > 0; o >>= 1) q += __shfl_xor(q, o);
  const float var = bfr(q * (1.0f / D_));
  const float m = bfr(mean);
  const float sd = bfr(sqrtf(bfr(var + 1e-5f)));
  u32x4 o0, o1;
#pragma unroll
  for (int i = 0; i < 4; ++i) {
    o0[i] = pk2(bfr(v[2 * i] - m) / sd, bfr(v[2 * i + 1] - m) / sd);
    o1[i] = pk2(bfr(v[8 + 2 * i] - m) / sd, bfr(v[8 + 2 * i + 1] - m) / sd);
  }
  *(u32x4*)(xn + atile_off(row, lane * 8, D_ / 32)) = o0;
  if (two) *(u32x4*)(xn + atile_off(row, 512 + lane * 8, D_ / 32)) = o1;
}

template <int WR, int WC, int RB, int NTW, int NG, int NSTAGE, int LW, bool XPF>
struct Gemm {
  static constexpr int NW = WR * WC;
  static constexpr int ROWS = WR * RB * 16;
  static constexpr int BSTEP = 4 * NG * 1024;
  static constexpr int ASTEP = ROWS * 64;
  static constexpr int STAGE = BSTEP + ASTEP;
  static constexpr int LDS = STAGE * NSTAGE;
  static constexpr int NBT = 4 * NG, NAT = ROWS / 16, NI = NBT + NAT;
  static constexpr int NGL = (NI + LW - 1) / LW;
  static constexpr int AHEAD = XPF ? 1 : 0;
  static_assert(NSTAGE >= 2 + AHEAD, "ring too short");

  template <typename WinFn, typename TileFn, typename UseFn>
  static DI void run(char* smem, const char* __restrict__ A, size_t rbs, int nk, WinFn winptr, TileFn tile, UseFn used,
                     f32x4 (&acc)[RB][NTW]) {
    const int tid = threadIdx.x, lane = tid & 63;
    const int w = __builtin_amdgcn_readfirstlane(tid >> 6);
    const int wr = w / WC;
    const bool loader = w < LW;
    const int lw = w % LW;
    const unsigned lds0 = lds_base(smem);
    const char* src[NGL]; int sstep[NGL]; unsigned sdst[NGL];
    unsigned vob[2][2];
    unsigned onmask = 0;
    int kind[NGL];
    const int brow = lane >> 3, bip = (lane & 7) >> 1, bhalf = lane & 1;
    int ld0 = 0;
    (void)winptr(0, ld0);
    int ld1 = ld0;
    (void)winptr(NG - 1, ld1);
#pragma unroll
    for (int kp = 0; kp < 2; ++kp) {
      const int swz = ((brow >> 1) & 1) | (kp << 1);
      vob[kp][0] = (unsigned)(brow * ld0 + 16 * (bip ^ swz) + 8 * bhalf) * 2u;
      vob[kp][1] = (unsigned)(brow * ld1 + 16 * (bip ^ swz) + 8 * bhalf) * 2u;
    }
    const unsigned voa = (unsigned)lane * 16u;
#pragma unroll
    for (int j = 0; j < NGL; ++j) {
      int id = lw + LW * j;
      if (id >= NI) id -= NI;
      if (id < NBT) {
        const int kb = id / NG, grp = id % NG;
        const int swz = ((brow >> 1) & 1) | ((kb & 1) << 1);
        int ld;
        const u16* p = winptr(grp, ld);
        src[j] = (const char*)(p + (size_t)(kb * 8) * ld);
        kind[j] = (kb & 1) * 2 + (ld == ld0 ? 0 : 1);
        if (used(grp * 4 + (bip ^ swz)) || lane == 0) onmask |= 1u << j;
        sstep[j] = 32 * ld * 2;
        sdst[j] = (unsigned)id * 1024u;
      } else {
        const int rb = id - NBT;
        src[j] = A + rb * rbs;
        kind[j] = 4;
        onmask |= 1u << j;
        sstep[j] = 1024;
        sdst[j] = (unsigned)(BSTEP + rb * 1024);
      }
    }
    unsigned ibuf = 0;
    auto issue1 = [&](int j) {
      const int kd = kind[j];
      const unsigned vo = kd == 4 ? voa : kd == 0 ? vob[0][0] : kd == 1 ? vob[0][1] : kd == 2 ? vob[1][0] : vob[1][1];
      if ((onmask >> j) & 1u) glds16s(src[j], vo, lds0 + ibuf + sdst[j]);
      src[j] += sstep[j];
    };
    auto issue_done = [&]() { ibuf = ibuf + STAGE == LDS ? 0u : ibuf + STAGE; };
    const int g = lane >> 4, q = (lane & 15) >> 2, p4 = lane & 3;
    const int bswz = ((q >> 1) & 1) | ((g & 1) << 1);
    int bo[NTW];
#pragma unroll
    for (int c = 0; c < NTW; ++c) {
      const int t = tile(c);
      bo[c] = g * NG * 1024 + q * 128 + (((t & 3) ^ bswz) * 32) + p4 * 8 + (t >> 2) * 1024;
    }
    const int ar = lane & 15;
    const int aoff = BSTEP + wr * RB * 1024 + ar * 64 + ((g ^ ((-(ar >> 2)) & 3)) * 16);

#pragma unroll
    for (int rb = 0; rb < RB; ++rb)
#pragma unroll
      for (int c = 0; c < NTW; ++c) acc[rb][c] = f32x4{0.f, 0.f, 0.f, 0.f};

    auto ldb = [&](const char* sbk, int c) -> s16x8 {
      const char* bp = sbk + bo[c];
      const s16x4 b0 = __builtin_amdgcn_ds_read_tr16_b64_v4i16((lds_s16x4_ptr)(bp));
      const s16x4 b1 = __builtin_amdgcn_ds_read_tr16_b64_v4i16((lds_s16x4_ptr)(bp + 512));
      return __builtin_shufflevector(b0, b1, 0, 1, 2, 3, 4, 5, 6, 7);
    };
    auto lda = [&](const char* sbk, s16x8 (&dst)[RB]) {
#pragma unroll
      for (int rb = 0; rb < RB; ++rb) dst[rb] = *(const s16x8*)(sbk + aoff + rb * 1024);
    };

    if (loader) {
#pragma unroll
      for (int s = 0; s < NSTAGE - 1; ++s) {
#pragma unroll
        for (int j = 0; j < NGL; ++j) issue1(j);
        issue_done();
      }
    }
    wait_vm_bar<(NSTAGE - 2 - AHEAD) * NGL>();
    s16x8 a[2][RB];
    s16x8 bA = ldb(smem, 0);
    lda(smem, a[0]);
    s16x8 bB = ldb(smem, NTW > 1 ? 1 : 0);

    unsigned cbuf = 0;
#pragma unroll 1
    for (int s0 = 0; s0 < nk; s0 += 2) {
#pragma unroll
      for (int u = 0; u < 2; ++u) {
        const int s = s0 + u;
        if (s >= nk) break;
        const bool do_issue = loader && s + NSTAGE - 1 < nk, has_next = s + 1 < nk;
        const char* sb = smem + cbuf;
        cbuf = cbuf + STAGE == LDS ? 0u : cbuf + STAGE;
        const char* nb = smem + cbuf;
        if (do_issue) {
#pragma unroll
          for (int j = 0; j < NGL; ++j) issue1(j);
          issue_done();
        }
        s16x8 bq[NTW + 2];
        bq[0] = bA; bq[1] = bB;
#pragma unroll
        for (int c = 0; c < NTW; ++c) {
          if (c + 2 < NTW) bq[c + 2] = ldb(sb, c + 2);
          else {
            bq[c + 2] = bq[c];
            if (XPF && has_next) {
              if (c + 2 == NTW || NTW == 1) lda(nb, a[u ^ 1]);
              bq[c + 2] = ldb(nb, (c + 2 - NTW) % NTW);
            }
          }
          __builtin_amdgcn_sched_barrier(0);
#pragma unroll
          for (int rb = 0; rb < RB; ++rb)
            acc[rb][c] = __builtin_amdgcn_mfma_f32_16x16x32_bf16(bq[c], a[u][rb], acc[rb][c], 0, 0, 0);
          __builtin_amdgcn_sched_barrier(0);
        }
        bA = bq[NTW]; bB = bq[NTW + 1];
        if (has_next) {
          if (s + NSTAGE - 1 < nk) wait_vm_bar<(NSTAGE - 2 - AHEAD) * NGL>(); else wait_vm_bar<0>();
          if (!XPF) {
            bA = ldb(nb, 0);
            lda(nb, a[u ^ 1]);
            bB = ldb(nb, NTW > 1 ? 1 : 0);
          }
        }
      }
    }
  }
};

constexpr int ROWB = 48;
constexpr int CH_K = 0, CH_V = 256 * ROWB, CH_BYTES = 512 * ROWB;
constexpr int IMG_HEAD = 8 * CH_BYTES;

typedef Gemm<4, 2, 4, 11, 8, 3, 4, true> G1;
constexpr float QSCALE = 0.23570226039551584f * 1.4426950408889634f;

DI float silu_mul(float gt, float up) {
  const float e = __builtin_amdgcn_exp2f(gt * -1.4426950408889634f);
  return gt * __builtin_amdgcn_rcpf(1.0f + e) * up;
}

__global__ __launch_bounds__(512, 2) void k_gemm1(const char* __restrict__ xn, const u16* __restrict__ wqkv,
                                                  const u16* __restrict__ w1, const u16* __restrict__ w3,
                                                  char* __restrict__ hbuf, u16* __restrict__ qbuf,
                                                  char* __restrict__ img) {
  extern __shared__ __attribute__((aligned(16))) char smem[];
  const int b = blockIdx.x;
  const int cgp = (b & 7) * 4 + ((b >> 3) & 3);
  const int panel = b >> 5;
  const int m0 = panel * 256, n0 = cgp * 144;
  const int qt0 = (108 * cgp) >> 5, qt1 = (108 * (cgp + 1)) >> 5;
  const int aw = n0 & ~63, toff = (n0 & 63) >> 4;
  const int qaw = (16 * qt0) & ~63, qoff = qt0 & 3;
  const int w = __builtin_amdgcn_readfirstlane(threadIdx.x >> 6);
  const int wr = w >> 1, wc = w & 1;
  auto winptr = [&](int g, int& ld) -> const u16* {
    if (g < 3) { ld = F_; return w1 + aw + 64 * g; }
    if (g < 6) { ld = F_; return w3 + aw + 64 * (g - 3); }
    ld = QKV_;
    const int c = qaw + 64 * (g - 6);
    return wqkv + (c < QKV_ ? c : qaw);
  };
  auto tile = [&](int c) -> int {
    if (wc == 0) return c < 5 ? toff + c : c < 10 ? 12 + toff + (c - 5) : 24 + qoff;
    const int t = c < 4 ? toff + 5 + c : c < 8 ? 12 + toff + 1 + c : 24 + qoff + 1 + (c - 8);
    return t < 32 ? t : 31;
  };
  auto used = [&](int t) -> bool {
    return t < 12 ? (unsigned)(t - toff) < 9u : t < 24 ? (unsigned)(t - 12 - toff) < 9u : (unsigned)(t - 24 - qoff) < (unsigned)(qt1 - qt0);
  };
  f32x4 acc[4][11];
  G1::run(smem, xn + (size_t)(m0 >> 4) * (D_ / 32) * 1024, (size_t)(D_ / 32) * 1024, D_ / 32, winptr, tile, used, acc);

  const int lane = threadIdx.x & 63, g = lane >> 4;
  const int row0 = m0 + 64 * wr + (lane & 15);
  if (wc == 0) {
#pragma unroll
    for (int c = 0; c < 5; ++c) {
      char* hp = hbuf + atile_off(row0, n0 + 16 * c + 4 * g, F_ / 32);
#pragma unroll
      for (int rb = 0; rb < 4; ++rb) {
        u32x2 o = {pk2(silu_mul(acc[rb][c][0], acc[rb][c + 5][0]), silu_mul(acc[rb][c][1], acc[rb][c + 5][1])),
                   pk2(silu_mul(acc[rb][c][2], acc[rb][c + 5][2]), silu_mul(acc[rb][c][3], acc[rb][c + 5][3]))};
        *(u32x2*)(hp + (size_t)rb * (F_ / 32) * 1024) = o;
      }
      __builtin_amdgcn_sched_barrier(0);
    }
  } else {
#pragma unroll
    for (int c = 0; c < 4; ++c) {
      char* hp = hbuf + atile_off(row0, n0 + 80 + 16 * c + 4 * g, F_ / 32);
#pragma unroll
      for (int rb = 0; rb < 4; ++rb) {
        u32x2 o = {pk2(silu_mul(acc[rb][c][0], acc[rb][c + 4][0]), silu_mul(acc[rb][c][1], acc[rb][c + 4][1])),
                   pk2(silu_mul(acc[rb][c][2], acc[rb][c + 4][2]), silu_mul(acc[rb][c][3], acc[rb][c + 4][3]))};
        *(u32x2*)(hp + (size_t)rb * (F_ / 32) * 1024) = o;
      }
      __builtin_amdgcn_sched_barrier(0);
    }
  }
#pragma unroll
  for (int c = 8; c < 11; ++c) {
    int t;
    if (wc == 0) { if (c != 10) continue; t = qt0; } else t = qt0 + 1 + (c - 8);
    if (t >= qt1) continue;
    const int which = t / 36;
    const int cc0 = 16 * (t - 36 * which) + 4 * g;
    const int head = cc0 / DH_, d0 = cc0 - head * DH_;
    const bool strad = d0 == 16;
    const int pos = d0 + 2 * (head & 1);
    const float sc = which == 0 ? QSCALE : 1.0f;
    char* base;
    size_t hstride;
    if (which == 0) { base = (char*)qbuf + (size_t)row0 * ROWB; hstride = (size_t)S_ * ROWB; }
    else { base = img + (row0 >> 8) * CH_BYTES + (which == 1 ? CH_K : CH_V) + (row0 & 255) * ROWB; hstride = IMG_HEAD; }
    char* p = base + head * hstride + pos * 2;
#pragma unroll
    for (int rb = 0; rb < 4; ++rb) {
      const unsigned lo = pk2(acc[rb][c][0] * sc, acc[rb][c][1] * sc), hi = pk2(acc[rb][c][2] * sc, acc[rb][c][3] * sc);
      u32x2 v = {lo, strad ? 0x3f803f80u : hi};
      *(u32x2*)(p + rb * 16 * ROWB) = v;
      if (strad) { u32x2 v2 = {0x3f803f80u, hi}; *(u32x2*)(base + (head + 1) * hstride + rb * 16 * ROWB) = v2; }
    }
  }
}

constexpr int AT_LDS = 3 * CH_BYTES;
constexpr float AT_GAP = 7.0f;

DI float max3f(float a, float b, float c) { return fmaxf(fmaxf(a, b), c); }
DI float rowmax16(const f32x16& s) {
  float mx = max3f(s[0], s[1], s[2]);
#pragma unroll
  for (int i = 3; i < 15; i += 2) mx = max3f(mx, s[i], s[i + 1]);
  return fmaxf(mx, s[15]);
}

template <int KG, int AT_NBUF>
DI void attn_block(char* smem, int ab, const char* __restrict__ qbuf, const char* __restrict__ img,
                   char* __restrict__ obuf) {
  const int tid = threadIdx.x, lane = tid & 63, r = lane & 31, hf = lane >> 5;
  const int wv = __builtin_amdgcn_readfirstlane(tid >> 6);
  const int w = wv & 7, kg = wv >> 3;
  constexpr int NT = 8 / KG, NP = 3 / KG + (3 % KG ? 1 : 0);
  const int head = (ab & 7) * 4 + ((ab >> 3) & 3), qb = ab >> 5, odd = head & 1;
  const unsigned lds0 = lds_base(smem);
  const char* gsrc = img + (size_t)head * IMG_HEAD;
  const unsigned vl = lane * 16;
  auto issue = [&](int ch) {
    const unsigned dst = lds0 + (unsigned)((ch % AT_NBUF) * CH_BYTES);
    const char* s = gsrc + (size_t)ch * CH_BYTES;
#pragma unroll
    for (int j = 0; j < NP; ++j) {
      int pc = wv + 8 * KG * j;
      if (pc >= 24) pc -= 24;
      glds16s(s + pc * 1024, vl, dst + pc * 1024);
    }
  };
#pragma unroll
  for (int c = 0; c < AT_NBUF - 1; ++c) issue(c);

  const int qrow = qb * 256 + w * 32 + r;
  const char* qp = qbuf + ((size_t)head * S_ + qrow) * ROWB;
  u32x4 q0u = *(const u32x4*)(qp + 16 * hf);
  u32x4 q1u = {0u, 0u, 0u, 0u};
  if (hf == 0) { const u32x2 t2 = *(const u32x2*)(qp + 32); q1u[0] = t2[0]; q1u[1] = t2[1]; }

  const int kmo = CH_K + r * ROWB + hf * 16;
  const int kto = CH_K + r * ROWB + 32;
  const int vo = CH_V + (4 * hf + ((lane & 15) >> 2)) * ROWB + ((lane >> 4) & 1) * 32 + (lane & 3) * 8;
  const f32x16 zero16 = {0.f, 0.f, 0.f, 0.f, 0.f, 0.f, 0.f, 0.f, 0.f, 0.f, 0.f, 0.f, 0.f, 0.f, 0.f, 0.f};

  struct KF { s16x8 f0; u32x2 tl; };
  auto kload = [&](const char* cb, int t) -> KF {
    KF k;
    k.f0 = *(const s16x8*)(cb + kmo + t * 32 * ROWB);
    k.tl = *(const u32x2*)(cb + kto + t * 32 * ROWB);
    return k;
  };
  auto qkm = [&](const KF& k) -> f32x16 {
    const u32x4 k1u = {k.tl[0], k.tl[1], 0u, 0u};
    f32x16 s = __builtin_amdgcn_mfma_f32_32x32x16_bf16(k.f0, __builtin_bit_cast(s16x8, q0u), zero16, 0, 0, 0);
    return __builtin_amdgcn_mfma_f32_32x32x16_bf16(__builtin_bit_cast(s16x8, k1u), __builtin_bit_cast(s16x8, q1u), s, 0, 0, 0);
  };
  auto vfrag = [&](const char* cb, int st) -> s16x8 {
    const char* p = cb + vo + st * 16 * ROWB;
    const s16x4 lo = __builtin_amdgcn_ds_read_tr16_b64_v4i16((lds_s16x4_ptr)(p));
    const s16x4 hi = __builtin_amdgcn_ds_read_tr16_b64_v4i16((lds_s16x4_ptr)(p + 8 * ROWB));
    return __builtin_shufflevector(lo, hi, 0, 1, 2, 3, 4, 5, 6, 7);
  };

  f32x16 o = zero16;
  float mrun = 0.f;
  auto set_m = [&](float m) {
    const float mh = bfr(m), ml = bfr(m - mh);
    mrun = mh + ml;
    const unsigned mm = pk2(-mh, -ml);
    if (hf == 0) { if (odd) q0u[0] = mm; else q1u[1] = mm; }
  };
  set_m(0.f);

  auto exps = [&](const f32x16& sx, u32x4& p0, u32x4& p1) {
#pragma unroll
    for (int i = 0; i < 4; ++i) p0[i] = pk2(__builtin_amdgcn_exp2f(sx[2 * i]), __builtin_amdgcn_exp2f(sx[2 * i + 1]));
#pragma unroll
    for (int i = 0; i < 4; ++i) p1[i] = pk2(__builtin_amdgcn_exp2f(sx[8 + 2 * i]), __builtin_amdgcn_exp2f(sx[8 + 2 * i + 1]));
  };
  auto bump = [&](f32x16& sx, bool first) -> float {
    const float mx = rowmax16(sx);
    float mq = fmaxf(mx, __shfl_xor(mx, 32)) + AT_GAP;
    if (!first) mq = fmaxf(mq, 0.f);
    const float mold = mrun;
    set_m(mold + mq);
    const float dl = mrun - mold;
    const float al = __builtin_amdgcn_exp2f(-dl);
#pragma unroll
    for (int i = 0; i < 16; ++i) { sx[i] -= dl; o[i] *= al; }
    return dl;
  };
#pragma unroll 1
  for (int ch = 0; ch < 8; ++ch) {
    if (ch + AT_NBUF - 2 < 8) wait_vm_bar<(AT_NBUF - 2) * NP>(); else wait_vm_bar<0>();
    if (ch + AT_NBUF - 1 < 8) issue(ch + AT_NBUF - 1);
    const char* cb = smem + (ch % AT_NBUF) * CH_BYTES + kg * NT * 32 * ROWB;
    KF kn = kload(cb, 1 < NT ? 1 : 0);
    f32x16 sc = qkm(kload(cb, 0));
    if (ch == 0) (void)bump(sc, true);
#pragma unroll
    for (int t = 0; t < NT; ++t) {
      f32x16 sn = zero16;
      if (t + 1 < NT) sn = qkm(kn);
      const s16x8 vf0 = vfrag(cb, 2 * t);
      const s16x8 vf1 = vfrag(cb, 2 * t + 1);
      if (t + 2 < NT) kn = kload(cb, t + 2);
      __builtin_amdgcn_sched_barrier(0);
      u32x4 p0, p1;
      exps(sc, p0, p1);
      const unsigned any = (p0[0] | p0[1] | p0[2]) | (p0[3] | p1[0] | p1[1]) | (p1[2] | p1[3]);
      if (__any((any & 0x40004000u) != 0u)) {
        const float dl = bump(sc, false);
#pragma unroll
        for (int i = 0; i < 16; ++i) sn[i] -= dl;
        exps(sc, p0, p1);
      }
      __builtin_amdgcn_sched_barrier(0);
      o = __builtin_amdgcn_mfma_f32_32x32x16_bf16(vf0, __builtin_bit_cast(s16x8, p0), o, 0, 0, 0);
      o = __builtin_amdgcn_mfma_f32_32x32x16_bf16(vf1, __builtin_bit_cast(s16x8, p1), o, 0, 0, 0);
      __builtin_amdgcn_sched_barrier(0);
      sc = sn;
    }
  }
  if (KG == 2) {
    __syncthreads();
    float* mb = (float*)smem + w * (17 * 64);
    if (kg == 1) {
#pragma unroll
      for (int i = 0; i < 16; ++i) mb[i * 64 + lane] = o[i];
      mb[16 * 64 + lane] = mrun;
    }
    __syncthreads();
    if (kg == 1) return;
    const float m1 = mb[16 * 64 + lane];
    const float mx = fmaxf(mrun, m1);
    const float a0 = __builtin_amdgcn_exp2f(mrun - mx), a1 = __builtin_amdgcn_exp2f(m1 - mx);
#pragma unroll
    for (int i = 0; i < 16; ++i) o[i] = o[i] * a0 + mb[i * 64 + lane] * a1;
  }
  float l = odd ? o[0] : o[10];
  l = __shfl(l, r);
  const float inv = 1.0f / l;
  const int c0 = head * DH_ + 4 * hf - 2 * odd;
  if (!(odd && hf == 0)) *(unsigned*)(obuf + atile_off(qrow, c0 + 0, D_ / 32)) = pk2(o[0] * inv, o[1] * inv);
  *(unsigned*)(obuf + atile_off(qrow, c0 + 2, D_ / 32)) = pk2(o[2] * inv, o[3] * inv);
  *(unsigned*)(obuf + atile_off(qrow, c0 + 8, D_ / 32)) = pk2(o[4] * inv, o[5] * inv);
  *(unsigned*)(obuf + atile_off(qrow, c0 + 10, D_ / 32)) = pk2(o[6] * inv, o[7] * inv);
  if (hf == 0) {
    *(unsigned*)(obuf + atile_off(qrow, c0 + 16, D_ / 32)) = pk2(o[8] * inv, o[9] * inv);
    if (odd) *(unsigned*)(obuf + atile_off(qrow, c0 + 18, D_ / 32)) = pk2(o[10] * inv, o[11] * inv);
  }
}

constexpr int DSPLIT = 4;
typedef Gemm<2, 4, 4, 3, 3, 4, 4, false> G2;
DI void down_block(char* smem, int b, const char* __restrict__ hbuf, const u16* __restrict__ w2,
                   char* __restrict__ part) {
  const int xcd = b & 7, idx = b >> 3;
  const int split = xcd >> 1, panel = (xcd & 1) * 8 + idx / 3, cg = idx % 3;
  const int m0 = panel * 128, n0 = cg * 192, k0 = split * (F_ / DSPLIT);
  const int w = __builtin_amdgcn_readfirstlane(threadIdx.x >> 6);
  const int wr = w >> 2, wc = w & 3;
  auto winptr = [&](int g, int& ld) -> const u16* { ld = D_; return w2 + (size_t)k0 * D_ + n0 + 64 * g; };
  auto tile = [&](int c) -> int { return wc * 3 + c; };
  f32x4 acc[4][3];
  G2::run(smem, hbuf + ((size_t)(m0 >> 4) * (F_ / 32) + (k0 >> 5)) * 1024, (size_t)(F_ / 32) * 1024, F_ / DSPLIT / 32,
          winptr, tile, [](int) { return true; }, acc);
  const int lane = threadIdx.x & 63;
#pragma unroll
  for (int rb = 0; rb < 4; ++rb) {
    const int rblk = (m0 >> 4) + 4 * wr + rb;
    char* pr = part + (((size_t)split * (S_ / 16) + rblk) * (D_ / 16) + (n0 >> 4) + 3 * wc) * 512 + lane * 8;
#pragma unroll
    for (int c = 0; c < 3; ++c) {
      u32x2 v = {pk2(acc[rb][c][0], acc[rb][c][1]), pk2(acc[rb][c][2], acc[rb][c][3])};
      *(u32x2*)(pr + 512 * c) = v;
    }
  }
}

constexpr int MID_LDS = AT_LDS > G2::LDS ? AT_LDS : G2::LDS;
__global__ __launch_bounds__(512, 4) void k_mid(const char* __restrict__ qbuf, const char* __restrict__ img,
                                                char* __restrict__ obuf, const char* __restrict__ hbuf,
                                                const u16* __restrict__ w2, char* __restrict__ part) {
  extern __shared__ __attribute__((aligned(16))) char smem[];
  if (blockIdx.x < 256) attn_block<1, 3>(smem, blockIdx.x, qbuf, img, obuf);
  else down_block(smem, blockIdx.x - 256, hbuf, w2, part);
}

constexpr int FIN_ST = 12288, FIN_LDS = 9 * FIN_ST;
DI void gload8g(u32x2& dst, const void* p) { asm volatile("global_load_dwordx2 %0, %1, off" : "=v"(dst) : "v"(p) : "memory"); }

__global__ __launch_bounds__(512, 2) void k_final(const char* __restrict__ obuf, const u16* __restrict__ wo,
                                                  const char* __restrict__ part, const u16* __restrict__ x,
                                                  u16* __restrict__ out) {
  extern __shared__ __attribute__((aligned(16))) char smem[];
  const int b = blockIdx.x;
  const int cg = (b >> 3) % 9, panel = (b & 7) * 2 + (b >> 3) / 9;
  const int m0 = panel * 128, n0 = cg * 64;
  const int lane = threadIdx.x & 63, g = lane >> 4;
  const int w = __builtin_amdgcn_readfirstlane(threadIdx.x >> 6);
  const int wr = w >> 1, wc = w & 1;
  const unsigned lds0 = lds_base(smem);
  const int brow = lane >> 3, bip = (lane & 7) >> 1, bhalf = lane & 1;
  const unsigned vob = (unsigned)(brow * D_ + 16 * (bip ^ (((brow >> 1) & 1) | ((w & 1) << 1))) + 8 * bhalf) * 2u;
  const unsigned voa = (unsigned)lane * 16u;
  const char* bsrc = (const char*)(wo + (size_t)((w & 3) * 8) * D_ + n0);
  const char* asrc = obuf + ((size_t)(m0 >> 4) + w) * (D_ / 32) * 1024;
  auto issue = [&](int s) {
    const unsigned dst = lds0 + (unsigned)((s % 9) * FIN_ST);
    glds16s(bsrc + (size_t)s * 32 * D_ * 2, vob, dst + (w & 3) * 1024);
    glds16s(asrc + (size_t)s * 1024, voa, dst + 4096 + w * 1024);
  };
#pragma unroll
  for (int s = 0; s < 6; ++s) issue(s);
  u32x2 pf[2][2][DSPLIT];
  u32x2 xv[2][2];
#pragma unroll
  for (int rb = 0; rb < 2; ++rb)
#pragma unroll
    for (int c = 0; c < 2; ++c) {
      const int row = m0 + 32 * wr + 16 * rb + (lane & 15), col = n0 + 32 * wc + 16 * c + 4 * g;
#pragma unroll
      for (int sp = 0; sp < DSPLIT; ++sp)
        gload8g(pf[rb][c][sp], part + (((size_t)sp * (S_ / 16) + (m0 >> 4) + 2 * wr + rb) * (D_ / 16) + (n0 >> 4) + 2 * wc + c) * 512 + lane * 8);
      gload8g(xv[rb][c], x + (size_t)row * D_ + col);
    }
  constexpr int NEX = 4 * (DSPLIT + 1);
  const int q = (lane & 15) >> 2, p4 = lane & 3;
  const int bswz = ((q >> 1) & 1) | ((g & 1) << 1);
  int bo[2];
#pragma unroll
  for (int c = 0; c < 2; ++c) bo[c] = g * 1024 + q * 128 + (((wc * 2 + c) ^ bswz) * 32) + p4 * 8;
  const int ar = lane & 15;
  const int aoff = 4096 + wr * 2048 + ar * 64 + ((g ^ ((-(ar >> 2)) & 3)) * 16);
  f32x4 acc[2][2];
#pragma unroll
  for (int rb = 0; rb < 2; ++rb)
#pragma unroll
    for (int c = 0; c < 2; ++c) acc[rb][c] = f32x4{0.f, 0.f, 0.f, 0.f};
#pragma unroll
  for (int gi = 0; gi < 6; ++gi) {
    if (gi == 0) wait_vm_bar<6 + NEX>(); else if (gi < 5) wait_vm_bar<6>(); else wait_vm_bar<0>();
    if (gi < 4) { issue(3 * gi + 6); issue(3 * gi + 7); issue(3 * gi + 8); }
#pragma unroll
    for (int u = 0; u < 3; ++u) {
      const char* sb = smem + ((3 * gi + u) % 9) * FIN_ST;
      s16x8 bf[2], af[2];
#pragma unroll
      for (int c = 0; c < 2; ++c) {
        const s16x4 b0 = __builtin_amdgcn_ds_read_tr16_b64_v4i16((lds_s16x4_ptr)(sb + bo[c]));
        const s16x4 b1 = __builtin_amdgcn_ds_read_tr16_b64_v4i16((lds_s16x4_ptr)(sb + bo[c] + 512));
        bf[c] = __builtin_shufflevector(b0, b1, 0, 1, 2, 3, 4, 5, 6, 7);
      }
#pragma unroll
      for (int rb = 0; rb < 2; ++rb) af[rb] = *(const s16x8*)(sb + aoff + rb * 1024);
#pragma unroll
      for (int rb = 0; rb < 2; ++rb)
#pragma unroll
        for (int c = 0; c < 2; ++c)
          acc[rb][c] = __builtin_amdgcn_mfma_f32_16x16x32_bf16(bf[c], af[rb], acc[rb][c], 0, 0, 0);
    }
  }
#pragma unroll
  for (int rb = 0; rb < 2; ++rb)
#pragma unroll
    for (int c = 0; c < 2; ++c) {
#pragma unroll
      for (int sp = 0; sp < DSPLIT; ++sp) asm volatile("" : "+v"(pf[rb][c][sp]));
      asm volatile("" : "+v"(xv[rb][c]));
    }
#pragma unroll
  for (int rb = 0; rb < 2; ++rb) {
#pragma unroll
    for (int c = 0; c < 2; ++c) {
      const int row = m0 + 32 * wr + 16 * rb + (lane & 15), col = n0 + 32 * wc + 16 * c + 4 * g;
      f32x4 f = {0.f, 0.f, 0.f, 0.f};
#pragma unroll
      for (int sp = 0; sp < DSPLIT; ++sp) {
        const u32x2 pv = pf[rb][c][sp];
        f += f32x4{__uint_as_float(pv[0] << 16), __uint_as_float(pv[0] & 0xffff0000u),
                   __uint_as_float(pv[1] << 16), __uint_as_float(pv[1] & 0xffff0000u)};
      }
      const u32x2 xw = xv[rb][c];
      float xf[4] = {__uint_as_float(xw[0] << 16), __uint_as_float(xw[0] & 0xffff0000u),
                     __uint_as_float(xw[1] << 16), __uint_as_float(xw[1] & 0xffff0000u)};
      float r4[4];
#pragma unroll
      for (int i = 0; i < 4; ++i) r4[i] = bfr(xf[i] + bfr(acc[rb][c][i])) + bfr(f[i]);
      u32x2 o = {pk2(r4[0], r4[1]), pk2(r4[2], r4[3])};
      *(u32x2*)(out + (size_t)row * D_ + col) = o;
    }
  }
}

constexpr size_t WS_XN = 0;
constexpr size_t WS_H = WS_XN + (size_t)S_ * D_ * 2;
constexpr size_t WS_Q = WS_H + (size_t)S_ * F_ * 2;
constexpr size_t WS_IMG = WS_Q + (size_t)H_ * S_ * ROWB;
constexpr size_t WS_O = WS_IMG + (size_t)H_ * IMG_HEAD;
constexpr size_t WS_P = WS_O + (size_t)S_ * D_ * 2;
constexpr size_t WS_END = WS_P + (size_t)DSPLIT * S_ * D_ * 2;

extern "C" void kernel_launch(void* const* d_in, const int* in_sizes, int n_in, void* d_out, int out_size,
                              void* d_ws, size_t ws_size, hipStream_t stream) {
  const u16* x = (const u16*)d_in[0];
  const u16* wqkv = (const u16*)d_in[1];
  const u16* wo = (const u16*)d_in[2];
  const u16* w1 = (const u16*)d_in[4];
  const u16* w3 = (const u16*)d_in[5];
  const u16* w2 = (const u16*)d_in[6];
  char* ws = (char*)d_ws;
  char* xn = ws + WS_XN;
  char* hbuf = ws + WS_H;
  u16* qbuf = (u16*)(ws + WS_Q);
  char* img = ws + WS_IMG;
  char* obuf = ws + WS_O;
  char* part = ws + WS_P;
  u16* out = (u16*)d_out;

  (void)hipFuncSetAttribute((const void*)k_gemm1, hipFuncAttributeMaxDynamicSharedMemorySize, G1::LDS);
  (void)hipFuncSetAttribute((const void*)k_mid, hipFuncAttributeMaxDynamicSharedMemorySize, MID_LDS);
  (void)hipFuncSetAttribute((const void*)k_final, hipFuncAttributeMaxDynamicSharedMemorySize, FIN_LDS);

  k_ln<<<S_ / 4 + 512, 256, 0, stream>>>(x, xn, wqkv, w1, w3);
  k_gemm1<<<256, 512, G1::LDS, stream>>>(xn, wqkv, w1, w3, hbuf, qbuf, img);
  k_mid<<<256 + 16 * 3 * DSPLIT, 512, MID_LDS, stream>>>((const char*)qbuf, img, obuf, hbuf, w2, part);
  k_final<<<16 * 9, 512, FIN_LDS, stream>>>(obuf, wo, part, x, out);
}
```
